```python
import torch

def _softmax(x, axis=-1):
    return torch.softmax(x, dim=axis)
def _gelu(x):
    return torch.nn.functional.gelu(x, approximate="tanh")
def _silu(x):
    return torch.nn.functional.silu(x)
def _sigmoid(x):
    return torch.sigmoid(x)
def _layernorm(x, axis=-1, eps=1e-5):
    m = x.mean(dim=axis, keepdim=True)
    v = x.var(dim=axis, keepdim=True, unbiased=False)
    return (x - m) / torch.sqrt(v + eps)
def _scatter_add(tgt, idx, src, axis=0):
    return tgt.clone().index_add_(axis, idx, src)
def _segment_sum(src, seg, n):
    out = torch.zeros((n,) + src.shape[1:], dtype=src.dtype)
    return out.index_add_(0, seg, src)
def _pad(x, pw):
    flat = tuple(v for pair in reversed(pw) for v in pair)
    return torch.nn.functional.pad(x, flat)

kernel_name = 'gated_mix__gqa_block_cached__glu_gated_reglu__decode'

def setup_inputs():

    _w = (lambda a: (lambda t: t.to(torch.bfloat16) if t.is_floating_point() else t)(torch.as_tensor(a)))
    def _A_setup():
        B, Sq, D = 1, 1, 1024
        Hq, Hkv, Sk, Dh = 32, 4, 65536, 128
        assert Hq % Hkv == 0, (Hq, Hkv)
        return {
            "x": _w((lambda *s: torch.randn(*s, dtype=torch.bfloat16) * 0.05)(B, Sq, D)),
            "k_cache": _w((lambda *s: torch.randn(*s, dtype=torch.bfloat16) * 3.2)(B, Hkv, Sk, Dh)),
            "v_cache": _w((lambda *s: torch.randn(*s, dtype=torch.bfloat16) * 1.6)(B, Hkv, Sk, Dh)),
            "wq": _w((lambda *s: torch.randn(*s, dtype=torch.bfloat16) * 3.2)(D, Hq * Dh) * (D ** -0.5)),
            "wo": _w((lambda *s: torch.randn(*s, dtype=torch.bfloat16) * 1.6)(Hq * Dh, D) * ((Hq * Dh) ** -0.5)),
        }
    _A_in = _A_setup()

    def _B_setup():
        B, S, D, F = 1, 1, 1024, 3584
        return {
            "x": _w((lambda *s: torch.randn(*s, dtype=torch.bfloat16) * 0.05)(B, S, D)),
            "W_gate": _w((lambda *s: torch.randn(*s, dtype=torch.bfloat16) * 1.6)(D, F) * (2.0 / D) ** 0.5),
            "W_up": _w((lambda *s: torch.randn(*s, dtype=torch.bfloat16) * 1.6)(D, F) * (2.0 / D) ** 0.5),
            "W_down": _w((lambda *s: torch.randn(*s, dtype=torch.bfloat16) * 1.6)(F, D) * (1.0 / F) ** 0.5),
        }
    _B_in = _B_setup()

    return {
        "A_x": _A_in["x"],
        "A_k_cache": _A_in["k_cache"],
        "A_v_cache": _A_in["v_cache"],
        "A_wq": _A_in["wq"],
        "A_wo": _A_in["wo"],
        "B_x": _B_in["x"],
        "B_W_gate": _B_in["W_gate"],
        "B_W_up": _B_in["W_up"],
        "B_W_down": _B_in["W_down"],
        "gate_w": _w((lambda *s: torch.randn(*s, dtype=torch.bfloat16) * 0.05)(1024) * 16.0),
    }

def reference(A_x, A_k_cache, A_v_cache, A_wq, A_wo, B_x, B_W_gate, B_W_up, B_W_down, gate_w):
    def _A_ref(x, k_cache, v_cache, wq, wo):
        B, Sq, D = x.shape
        Hq, Dh = 32, 128
        Hkv = k_cache.shape[1]
        rep = Hq // Hkv
        q = (lambda x, *d: x.permute(*d))((x @ wq).reshape(B, Sq, Hq, Dh), 0, 2, 1, 3)
        k = (lambda x, n, axis: torch.repeat_interleave(x, n, dim=axis))(k_cache, rep, axis=1)
        v = (lambda x, n, axis: torch.repeat_interleave(x, n, dim=axis))(v_cache, rep, axis=1)
        scores = torch.einsum("bhqd,bhkd->bhqk", q, k) * (Dh ** -0.5)
        p = _softmax(scores, axis=-1)
        o = torch.einsum("bhqk,bhkd->bhqd", p, v)
        return (lambda x, *d: x.permute(*d))(o, 0, 2, 1, 3).reshape(B, Sq, Hq * Dh) @ wo

    def _B_ref(x, W_gate, W_up, W_down):
        gate = torch.einsum("bsd,df->bsf", x, W_gate)
        up = torch.einsum("bsd,df->bsf", x, W_up)
        gate = gate * (lambda x, dt: x.to(dt))(gate > 0, gate.dtype)
        h = gate * up
        return torch.einsum("bsf,fd->bsd", h, W_down)

    x = A_x
    g = _sigmoid((x * gate_w).sum(-1, keepdims=True))
    a = _A_ref(x, A_k_cache, A_v_cache, A_wq, A_wo)
    b = _B_ref(x, B_W_gate, B_W_up, B_W_down)
    return g * a + (1 - g) * b
```

```cpp
#include <hip/hip_runtime.h>
#include <hip/hip_bf16.h>
#include <stdint.h>

typedef unsigned short u16;
typedef unsigned v4u __attribute__((ext_vector_type(4)));
typedef float f32x4 __attribute__((ext_vector_type(4)));

constexpr int D = 1024, HQ = 32, HKV = 4, SK = 65536, DH = 128, F = 3584;
constexpr int NSPLIT = 64;
constexpr int KPS = SK / NSPLIT;

__device__ __forceinline__ float bf2f(unsigned b) { return __uint_as_float(b << 16); }
__device__ __forceinline__ unsigned f2bf(float f) { unsigned u = __float_as_uint(f); u += 0x7FFFu + ((u >> 16) & 1u); return u >> 16; }
__device__ __forceinline__ float rbf(float f) { return bf2f(f2bf(f)); }

__global__ void __launch_bounds__(256) k1(const u16* x, const u16* wq, const u16* wg, const u16* wu, float* q, float* h) {
  __shared__ float red[2][4][64];
  const int tid = threadIdx.x, c = tid & 63, rq = tid >> 6;
  const int b = blockIdx.x;
  if (b < 64) {
    int col = b * 64 + c;
    float acc = 0.f;
    for (int r = rq * 256; r < rq * 256 + 256; ++r) acc += bf2f(x[r]) * bf2f(wq[(size_t)r * 4096 + col]);
    red[0][rq][c] = acc;
    __syncthreads();
    if (rq == 0) q[col] = rbf(red[0][0][c] + red[0][1][c] + red[0][2][c] + red[0][3][c]);
  } else {
    int col = (b - 64) * 64 + c;
    float ag = 0.f, au = 0.f;
    for (int r = rq * 256; r < rq * 256 + 256; ++r) { float xv = bf2f(x[r]); ag += xv * bf2f(wg[(size_t)r * F + col]); au += xv * bf2f(wu[(size_t)r * F + col]); }
    red[0][rq][c] = ag; red[1][rq][c] = au;
    __syncthreads();
    if (rq == 0) {
      float g = rbf(red[0][0][c] + red[0][1][c] + red[0][2][c] + red[0][3][c]);
      float u = rbf(red[1][0][c] + red[1][1][c] + red[1][2][c] + red[1][3][c]);
      g = g > 0.f ? g : 0.f;
      h[col] = rbf(g * u);
    }
  }
}

__global__ void __launch_bounds__(256) k2(const float* q, const u16* K, const u16* V, float* pm, float* pl, float* po) {
  __shared__ float qs[8][128];
  __shared__ float s[8][KPS];
  __shared__ float mm[8], ll[8];
  const int tid = threadIdx.x, b = blockIdx.x, g = b / NSPLIT, sp = b % NSPLIT;
  for (int i = tid; i < 1024; i += 256) qs[i >> 7][i & 127] = q[g * 1024 + i];
  __syncthreads();
  const u16* Kb = K + ((size_t)g * SK + (size_t)sp * KPS) * DH;
  const u16* Vb = V + ((size_t)g * SK + (size_t)sp * KPS) * DH;
  for (int k = tid; k < KPS; k += 256) {
    float acc[8];
    for (int hh = 0; hh < 8; ++hh) acc[hh] = 0.f;
    const v4u* row = (const v4u*)(Kb + (size_t)k * DH);
    for (int c = 0; c < 16; ++c) {
      v4u w = row[c];
      float f[8];
      for (int j = 0; j < 4; ++j) { f[2 * j] = __uint_as_float(w[j] << 16); f[2 * j + 1] = __uint_as_float(w[j] & 0xffff0000u); }
      for (int hh = 0; hh < 8; ++hh) for (int j = 0; j < 8; ++j) acc[hh] += f[j] * qs[hh][c * 8 + j];
    }
    for (int hh = 0; hh < 8; ++hh) s[hh][k] = acc[hh] * 0.08838834764831845f;
  }
  __syncthreads();
  {
    int hh = tid >> 5, l = tid & 31;
    float m = -INFINITY;
    for (int k = l; k < KPS; k += 32) m = fmaxf(m, s[hh][k]);
    for (int o = 16; o >= 1; o >>= 1) m = fmaxf(m, __shfl_xor(m, o));
    float sum = 0.f;
    for (int k = l; k < KPS; k += 32) { float p = __expf(s[hh][k] - m); s[hh][k] = p; sum += p; }
    for (int o = 16; o >= 1; o >>= 1) sum += __shfl_xor(sum, o);
    if (l == 0) { mm[hh] = m; ll[hh] = sum; }
  }
  __syncthreads();
  {
    int d = tid & 127, hq = tid >> 7;
    float acc[4] = {0.f, 0.f, 0.f, 0.f};
    for (int k = 0; k < KPS; ++k) {
      float v = bf2f(Vb[(size_t)k * DH + d]);
      for (int j = 0; j < 4; ++j) acc[j] += s[hq * 4 + j][k] * v;
    }
    for (int j = 0; j < 4; ++j) po[((size_t)(g * 8 + hq * 4 + j) * NSPLIT + sp) * DH + d] = acc[j];
    if (tid < 8) { pm[(g * 8 + tid) * NSPLIT + sp] = mm[tid]; pl[(g * 8 + tid) * NSPLIT + sp] = ll[tid]; }
  }
}

__global__ void __launch_bounds__(128) k3(const float* pm, const float* pl, const float* po, float* o) {
  const int hh = blockIdx.x, d = threadIdx.x;
  float M = -INFINITY;
  for (int s = 0; s < NSPLIT; ++s) M = fmaxf(M, pm[hh * NSPLIT + s]);
  float L = 0.f, acc = 0.f;
  for (int s = 0; s < NSPLIT; ++s) { float w = __expf(pm[hh * NSPLIT + s] - M); L += w * pl[hh * NSPLIT + s]; acc += w * po[((size_t)hh * NSPLIT + s) * DH + d]; }
  o[hh * DH + d] = rbf(acc / L);
}

__global__ void __launch_bounds__(1024) k4(const u16* x, const u16* gw, const float* o, const float* h, const u16* wo, const u16* wd, u16* out) {
  __shared__ float ra[16][64], rb[16][64];
  __shared__ float gs[16];
  const int tid = threadIdx.x, c = tid & 63, rg = tid >> 6, col = blockIdx.x * 64 + c;
  float a = 0.f, bb = 0.f;
  for (int r = rg * 256; r < rg * 256 + 256; ++r) a += o[r] * bf2f(wo[(size_t)r * D + col]);
  for (int r = rg * 224; r < rg * 224 + 224; ++r) bb += h[r] * bf2f(wd[(size_t)r * D + col]);
  ra[rg][c] = a; rb[rg][c] = bb;
  float t = rbf(bf2f(x[tid]) * bf2f(gw[tid]));
  for (int of = 32; of >= 1; of >>= 1) t += __shfl_xor(t, of);
  if (c == 0) gs[rg] = t;
  __syncthreads();
  if (rg == 0) {
    float sa = 0.f, sb = 0.f, sg = 0.f;
    for (int i = 0; i < 16; ++i) { sa += ra[i][c]; sb += rb[i][c]; sg += gs[i]; }
    sg = rbf(sg);
    float g = rbf(1.f / (1.f + __expf(-sg)));
    float r = rbf(rbf(g * rbf(sa)) + rbf(rbf(1.f - g) * rbf(sb)));
    out[col] = (u16)f2bf(r);
  }
}

extern "C" void kernel_launch(void* const* d_in, const int* in_sizes, int n_in, void* d_out, int out_size, void* d_ws, size_t ws_size, hipStream_t stream) {
  const u16* x = (const u16*)d_in[0];
  const u16* K = (const u16*)d_in[1];
  const u16* V = (const u16*)d_in[2];
  const u16* wq = (const u16*)d_in[3];
  const u16* wo = (const u16*)d_in[4];
  const u16* wg = (const u16*)d_in[6];
  const u16* wu = (const u16*)d_in[7];
  const u16* wd = (const u16*)d_in[8];
  const u16* gw = (const u16*)d_in[9];
  float* ws = (float*)d_ws;
  float* q = ws; float* h = ws + 4096; float* o = ws + 8192; float* pm = ws + 16384; float* pl = pm + 32 * NSPLIT; float* po = pl + 32 * NSPLIT;
  k1<<<64 + 56, 256, 0, stream>>>(x, wq, wg, wu, q, h);
  k2<<<256, 256, 0, stream>>>(q, K, V, pm, pl, po);
  k3<<<32, 128, 0, stream>>>(pm, pl, po, o);
  k4<<<16, 1024, 0, stream>>>(x, gw, o, h, wo, wd, (u16*)d_out);
}
```

```cpp
#include <hip/hip_runtime.h>
#include <stdint.h>

namespace fk {

typedef unsigned v4u __attribute__((ext_vector_type(4)));
typedef unsigned v2u __attribute__((ext_vector_type(2)));
typedef float f32x4 __attribute__((ext_vector_type(4)));
typedef float f32x2 __attribute__((ext_vector_type(2)));
typedef __bf16 bf16x8 __attribute__((ext_vector_type(8)));
typedef __bf16 bf16x2 __attribute__((ext_vector_type(2)));
#define LAS __attribute__((address_space(3)))
#define GAS __attribute__((address_space(1)))

constexpr int NWG = 256, NTHR = 512;
constexpr int FF = 3584;
constexpr int WS_EPOCH = 0;
constexpr int WS_QP = 4096;
constexpr int WS_GU = WS_QP + 4 * 4096 * 4;
constexpr int WS_ML = WS_GU + 2 * 8 * FF * 4;
constexpr int WS_OP = WS_ML + 32 * 64 * 16;
constexpr int WS_FP = WS_OP + 32 * 2 * 64 * 64 * 4;
constexpr int WS_END = WS_FP + 4 * 64 * 256 * 4;
constexpr int L_KB = 0;
constexpr int L_RQ = 65536;
constexpr int L_RG = L_RQ + 2048;
constexpr int L_RC = L_RG + 8192;
constexpr int L_RF = L_RC + 2048;
constexpr int L_QS = L_RF + 8192;
constexpr int L_MS = L_QS + 2048;
constexpr int L_GS = L_MS + 512;
constexpr int L_HS = L_GS + 1024;
constexpr int L_GM = L_HS + 256;
constexpr int L_END = L_GM + 64;
constexpr int LDS_BYTES = 90112;
static_assert(L_END <= LDS_BYTES, "lds");

constexpr int AUX_NT = 2, AUX_SC1 = 16;
constexpr unsigned SPIN_CAP = 1u << 18;
constexpr float SCL = 0.08838834764831845f * 1.4426950408889634f;

#define STAMP(k) do { } while (0)

struct Params {
  const void *x, *K, *V, *wq, *wo, *wg, *wu, *wd, *gw;
  void* out;
  unsigned char* ws;
};

__device__ __forceinline__ float bflo(unsigned u) { return __uint_as_float(u << 16); }
__device__ __forceinline__ float bfhi(unsigned u) { return __uint_as_float(u & 0xffff0000u); }
__device__ __forceinline__ unsigned pkbf(float a, float b) { f32x2 v = {a, b}; return __builtin_bit_cast(unsigned, __builtin_convertvector(v, bf16x2)); }
__device__ __forceinline__ float rbf(float a) { return bflo(pkbf(a, 0.f)); }
template <int AUX> __device__ __forceinline__ v4u ld16(__amdgpu_buffer_rsrc_t rs, int voff, int soff) { return __builtin_bit_cast(v4u, __builtin_amdgcn_raw_buffer_load_b128(rs, voff, soff, AUX)); }
template <int AUX> __device__ __forceinline__ v2u ld8(__amdgpu_buffer_rsrc_t rs, int voff, int soff) { return __builtin_bit_cast(v2u, __builtin_amdgcn_raw_buffer_load_b64(rs, voff, soff, AUX)); }
template <int AUX> __device__ __forceinline__ unsigned ld4(__amdgpu_buffer_rsrc_t rs, int voff, int soff) { return __builtin_amdgcn_raw_buffer_load_b32(rs, voff, soff, AUX); }
__device__ __forceinline__ void st16(__amdgpu_buffer_rsrc_t rs, int voff, v4u v) { __builtin_amdgcn_raw_buffer_store_b128(v, rs, voff, 0, AUX_SC1); }
#define RSRC(p, n) __builtin_amdgcn_make_buffer_rsrc((void*)(p), 0, (n), 0x00020000)
#define SCHED() __builtin_amdgcn_sched_barrier(0)
#define BARRIER() asm volatile("s_waitcnt lgkmcnt(0)\n\ts_barrier" ::: "memory")
#define MEMFENCE() asm volatile("" ::: "memory")

__device__ __forceinline__ float swap_add32(float a, float b) {
  auto s = __builtin_amdgcn_permlane32_swap(__float_as_uint(a), __float_as_uint(b), false, false);
  return __uint_as_float(s[0]) + __uint_as_float(s[1]);
}
__device__ __forceinline__ float swap_add16(float a, float b) {
  auto s = __builtin_amdgcn_permlane16_swap(__float_as_uint(a), __float_as_uint(b), false, false);
  return __uint_as_float(s[0]) + __uint_as_float(s[1]);
}
template <int CTRL> __device__ __forceinline__ float dpp(float x) { return __builtin_bit_cast(float, __builtin_amdgcn_mov_dpp(__builtin_bit_cast(int, x), CTRL, 0xf, 0xf, true)); }
constexpr int ROR8 = 0x128, ROR4 = 0x124, ROR2 = 0x122, ROR1 = 0x121;
__device__ __forceinline__ float xrow_max(float x) {
  auto s = __builtin_amdgcn_permlane16_swap(__float_as_uint(x), __float_as_uint(x), false, false);
  x = fmaxf(__uint_as_float(s[0]), __uint_as_float(s[1]));
  auto t = __builtin_amdgcn_permlane32_swap(__float_as_uint(x), __float_as_uint(x), false, false);
  return fmaxf(__uint_as_float(t[0]), __uint_as_float(t[1]));
}
__device__ __forceinline__ float xrow_sum(float x) {
  auto s = __builtin_amdgcn_permlane16_swap(__float_as_uint(x), __float_as_uint(x), false, false);
  x = __uint_as_float(s[0]) + __uint_as_float(s[1]);
  auto t = __builtin_amdgcn_permlane32_swap(__float_as_uint(x), __float_as_uint(x), false, false);
  return __uint_as_float(t[0]) + __uint_as_float(t[1]);
}
__device__ __forceinline__ float wave_sum(float x) { x += dpp<ROR8>(x); x += dpp<ROR4>(x); x += dpp<ROR2>(x); x += dpp<ROR1>(x); return xrow_sum(x); }
__device__ __forceinline__ float wave_max(float x) { x = fmaxf(x, dpp<ROR8>(x)); x = fmaxf(x, dpp<ROR4>(x)); x = fmaxf(x, dpp<ROR2>(x)); x = fmaxf(x, dpp<ROR1>(x)); return xrow_max(x); }
__device__ __forceinline__ float reduce8(const float (&a)[8], int lane) {
  float t[4], u[2];
#pragma unroll
  for (int j = 0; j < 4; ++j) t[j] = swap_add32(a[j], a[j + 4]);
#pragma unroll
  for (int j = 0; j < 2; ++j) u[j] = swap_add16(t[j], t[j + 2]);
  const float v0 = u[0] + dpp<ROR8>(u[0]), v1 = u[1] + dpp<ROR8>(u[1]);
  return (lane & 8) ? v1 : v0;
}
__device__ __forceinline__ void fma8(float (&acc)[8], float xv, v4u w) {
#pragma unroll
  for (int j = 0; j < 4; ++j) { acc[2 * j] = fmaf(xv, bflo(w[j]), acc[2 * j]); acc[2 * j + 1] = fmaf(xv, bfhi(w[j]), acc[2 * j + 1]); }
}
struct Tags { unsigned t0, t1, t2, t3; };
__device__ __forceinline__ unsigned enc1(float v, unsigned t) { return ((__float_as_uint(v) + 512u) & ~1023u) | t; }
__device__ __forceinline__ v4u enc4(f32x4 v, const Tags& t) { return v4u{enc1(v.x, t.t0), enc1(v.y, t.t1), enc1(v.z, t.t2), enc1(v.w, t.t3)}; }
__device__ __forceinline__ unsigned bad4(v4u v, const Tags& t) { return (((v.x ^ t.t0) | (v.y ^ t.t1)) | ((v.z ^ t.t2) | (v.w ^ t.t3))) & 1023u; }
__device__ __forceinline__ f32x4 dec4(v4u v) { return f32x4{__uint_as_float(v.x & ~1023u), __uint_as_float(v.y & ~1023u), __uint_as_float(v.z & ~1023u), __uint_as_float(v.w & ~1023u)}; }
__device__ __forceinline__ bool wave_ok(unsigned bad) { return __builtin_amdgcn_ballot_w64(bad != 0u) == 0ull; }
__device__ __forceinline__ f32x4 mfma(v4u a, v4u b, f32x4 c) { return __builtin_amdgcn_mfma_f32_16x16x32_bf16(__builtin_bit_cast(bf16x8, a), __builtin_bit_cast(bf16x8, b), c, 0, 0, 0); }

#define LDS4(off) (*(LAS f32x4*)(L + (off)))
#define LDSU4(off) (*(LAS v4u*)(L + (off)))
#define LDS1(off) (*(LAS float*)(L + (off)))

__global__ void __launch_bounds__(NTHR, 2) fused_kernel(Params p) {
  extern __shared__ __attribute__((aligned(16))) unsigned char smem[];
  LAS unsigned char* L = (LAS unsigned char*)smem;
  const int tid = threadIdx.x, lane = tid & 63, w = __builtin_amdgcn_readfirstlane(tid >> 6), b = blockIdx.x;
  STAMP(0);
  const __amdgpu_buffer_rsrc_t rX = RSRC(p.x, 2048), rGW = RSRC(p.gw, 2048), rK = RSRC(p.K, 64 << 20), rV = RSRC(p.V, 64 << 20), rQ = RSRC(p.wq, 8 << 20), rO = RSRC(p.wo, 8 << 20),
                                rG = RSRC(p.wg, 7 << 20), rU = RSRC(p.wu, 7 << 20), rD = RSRC(p.wd, 7 << 20), rW = RSRC(p.ws, WS_END), rOut = RSRC(p.out, 2048);
  const int q_cg = b & 63, q_ks = b >> 6;
  const bool mlp = b < 224;
  const int g_cg = b % 28, g_ks = b / 28;
  const int hd = (b >> 3) & 3, sp = (b >> 5) * 8 + (b & 7);
  const int rg = (b & 7) + 8 * (b >> 5), cg4 = (b >> 3) & 3;
  constexpr int OOB = 0x7ffffff0;

  if (b < 8 && w == 7) { __builtin_amdgcn_fence(__ATOMIC_RELEASE, "agent"); asm volatile("s_waitcnt vmcnt(0)" ::: "memory"); }
  const unsigned E = ld4<AUX_SC1>(rW, 0, WS_EPOCH);
  const v2u xq = ld8<0>(rX, (q_ks * 256 + w * 32 + (lane >> 3) * 4) * 2, 0);
  const v2u xg = ld8<0>(rX, ((g_ks & 7) * 128 + w * 16 + (lane >> 4) * 4) * 2, 0);
  const unsigned xm = ld4<0>(rX, (w * 128 + lane * 2) * 2, 0), gm = ld4<0>(rGW, (w * 128 + lane * 2) * 2, 0);
  const int qv = ((q_ks * 256 + w * 32 + (lane >> 3) * 4) * 4096 + q_cg * 64 + (lane & 7) * 8) * 2;
  v4u wqv[4];
#pragma unroll
  for (int n = 0; n < 4; ++n) wqv[n] = ld16<AUX_NT>(rQ, qv, n * 8192);
  const int gv = mlp ? ((g_ks * 128 + w * 16 + (lane >> 4) * 4) * FF + g_cg * 128 + (lane & 15) * 8) * 2 : OOB;
  v4u gtv[4], upv[4];
  SCHED();
  asm volatile("s_barrier" ::: "memory");
#pragma unroll
  for (int n = 0; n < 4; ++n) gtv[n] = ld16<AUX_NT>(rG, gv, n * FF * 2);
  SCHED();
  {
    float acc[8] = {0.f, 0.f, 0.f, 0.f, 0.f, 0.f, 0.f, 0.f};
    fma8(acc, bflo(xq.x), wqv[0]); fma8(acc, bfhi(xq.x), wqv[1]); fma8(acc, bflo(xq.y), wqv[2]); fma8(acc, bfhi(xq.y), wqv[3]);
    STAMP(1);
    const float r = reduce8(acc, lane);
    LDS1(L_RQ + (w * 64 + (lane & 7) * 8 + (lane >> 3)) * 4) = r;
  }
  SCHED();
#pragma unroll
  for (int n = 0; n < 4; ++n) upv[n] = ld16<AUX_NT>(rU, gv, n * FF * 2);
  SCHED();
  const unsigned T = E + 1u;
  Tags tg;
  tg.t0 = T & 1023u; tg.t1 = (T + (T >> 10)) & 1023u; tg.t2 = (T + (T >> 20)) & 1023u; tg.t3 = (T + (T >> 30) + 341u) & 1023u;
  {
    const float s = wave_sum(rbf(bflo(xm) * bflo(gm)) + rbf(bfhi(xm) * bfhi(gm)));
    if (lane == 0) LDS1(L_GM + w * 4) = s;
  }
  BARRIER();
  if (w == 0 && lane < 16) {
    f32x4 s = LDS4(L_RQ + lane * 16);
#pragma unroll
    for (int ww = 1; ww < 8; ++ww) s += LDS4(L_RQ + ww * 256 + lane * 16);
    st16(rW, WS_QP + (q_ks * 4096 + q_cg * 64 + lane * 4) * 4, enc4(s, tg));
    STAMP(2);
  }
  float gmix;
  {
    const f32x4 a = LDS4(L_GM), c = LDS4(L_GM + 16);
    const float sg = rbf(((a.x + a.y) + (a.z + a.w)) + ((c.x + c.y) + (c.z + c.w)));
    gmix = rbf(1.f / (1.f + __expf(-sg)));
  }
  const int kvb = hd * (16 << 20) + (sp * 8 + w) * 8192;
  constexpr int TSTEP = 512 * 8192;
  v4u kt0[8], kt1[8];
  {
    float ag[8] = {0.f, 0.f, 0.f, 0.f, 0.f, 0.f, 0.f, 0.f}, au[8] = {0.f, 0.f, 0.f, 0.f, 0.f, 0.f, 0.f, 0.f};
    fma8(ag, bflo(xg.x), gtv[0]); fma8(ag, bfhi(xg.x), gtv[1]); fma8(ag, bflo(xg.y), gtv[2]); fma8(ag, bfhi(xg.y), gtv[3]);
    SCHED();
#pragma unroll
    for (int j = 0; j < 4; ++j) kt0[j] = ld16<AUX_NT>(rK, lane * 16, kvb + j * 1024);
    SCHED();
    fma8(au, bflo(xg.x), upv[0]); fma8(au, bfhi(xg.x), upv[1]); fma8(au, bflo(xg.y), upv[2]); fma8(au, bfhi(xg.y), upv[3]);
    SCHED();
#pragma unroll
    for (int j = 4; j < 8; ++j) kt0[j] = ld16<AUX_NT>(rK, lane * 16, kvb + j * 1024);
    SCHED();
    float t[4], u[2];
#pragma unroll
    for (int j = 0; j < 4; ++j) t[j] = swap_add32(ag[j], ag[j + 4]);
#pragma unroll
    for (int j = 0; j < 2; ++j) u[j] = swap_add16(t[j], t[j + 2]);
    *(LAS f32x2*)(L + L_RG + ((w * 2 + 0) * 128 + (lane & 15) * 8 + (lane >> 4) * 2) * 4) = f32x2{u[0], u[1]};
#pragma unroll
    for (int j = 0; j < 4; ++j) t[j] = swap_add32(au[j], au[j + 4]);
#pragma unroll
    for (int j = 0; j < 2; ++j) u[j] = swap_add16(t[j], t[j + 2]);
    *(LAS f32x2*)(L + L_RG + ((w * 2 + 1) * 128 + (lane & 15) * 8 + (lane >> 4) * 2) * 4) = f32x2{u[0], u[1]};
  }
  BARRIER();
  if (w == 1 && mlp) {
    const int mat = lane >> 5, pc = lane & 31;
    f32x4 s = LDS4(L_RG + (mat * 128 + pc * 4) * 4);
#pragma unroll
    for (int ww = 1; ww < 8; ++ww) s += LDS4(L_RG + ((ww * 2 + mat) * 128 + pc * 4) * 4);
    st16(rW, WS_GU + ((mat * 8 + g_ks) * FF + g_cg * 128 + pc * 4) * 4, enc4(s, tg));
    STAMP(3);
  }
  {
    const int qo = WS_QP + ((lane >> 5) * 2 * 4096 + (hd * 8 + w) * 128 + (lane & 31) * 4) * 4;
    v4u q0 = ld16<AUX_SC1>(rW, qo, 0), q1 = ld16<AUX_SC1>(rW, qo, 4096 * 4);
    SCHED();
#pragma unroll
    for (int j = 0; j < 8; ++j) kt1[j] = ld16<AUX_NT>(rK, lane * 16, kvb + TSTEP + j * 1024);
    SCHED();
    if (!wave_ok(bad4(q0, tg) | bad4(q1, tg))) {
      for (unsigned spins = 0; spins < SPIN_CAP; ++spins) {
        __builtin_amdgcn_s_sleep(1);
        MEMFENCE();
        q0 = ld16<AUX_SC1>(rW, qo, 0); q1 = ld16<AUX_SC1>(rW, qo, 4096 * 4);
        if (wave_ok(bad4(q0, tg) | bad4(q1, tg))) break;
      }
    }
    const f32x4 a = dec4(q0) + dec4(q1);
    f32x4 s;
    s.x = swap_add32(a.x, a.x); s.y = swap_add32(a.y, a.y); s.z = swap_add32(a.z, a.z); s.w = swap_add32(a.w, a.w);
    if (lane < 32) *(LAS v2u*)(L + L_QS + (w * 128 + lane * 4) * 2) = v2u{pkbf(s.x, s.y), pkbf(s.z, s.w)};
  }
  BARRIER();
  STAMP(4);
  const int g4 = lane >> 4, n16 = lane & 15;
  v4u qf[4];
#pragma unroll
  for (int s = 0; s < 4; ++s) qf[s] = LDSU4(L_QS + ((n16 & 7) * 128 + (4 * s + g4) * 8) * 2);
  f32x4 S[8];
  const int kb = L_KB + w * 8192;
  auto qk_tile = [&](const v4u (&kt)[8], int t) {
#pragma unroll
    for (int j = 0; j < 8; ++j) { const int r = 4 * g4 + (j & 3); LDSU4(kb + ((j >> 2) * 16 + r) * 256 + ((n16 ^ r) << 4)) = kt[j]; }
#pragma unroll
    for (int T2 = 0; T2 < 2; ++T2) {
      f32x4 c = {0.f, 0.f, 0.f, 0.f};
#pragma unroll
      for (int s = 0; s < 4; ++s) c = mfma(LDSU4(kb + (T2 * 16 + n16) * 256 + (((4 * s + g4) ^ n16) << 4)), qf[s], c);
      S[2 * t + T2] = c;
    }
  };
  v4u kt2[8], kt3[8], vt0[8], vt1[8], vt2[8], vt3[8];
  qk_tile(kt0, 0);
  SCHED();
#pragma unroll
  for (int j = 0; j < 8; ++j) kt2[j] = ld16<AUX_NT>(rK, lane * 16, kvb + 2 * TSTEP + j * 1024);
  SCHED();
  qk_tile(kt1, 1);
  SCHED();
#pragma unroll
  for (int j = 0; j < 8; ++j) kt3[j] = ld16<AUX_NT>(rK, lane * 16, kvb + 3 * TSTEP + j * 1024);
  SCHED();
  qk_tile(kt2, 2);
  SCHED();
#pragma unroll
  for (int j = 0; j < 8; ++j) vt0[j] = ld16<AUX_NT>(rV, lane * 16, kvb + j * 1024);
  SCHED();
  qk_tile(kt3, 3);
  SCHED();
#pragma unroll
  for (int j = 0; j < 8; ++j) vt1[j] = ld16<AUX_NT>(rV, lane * 16, kvb + TSTEP + j * 1024);
  SCHED();
  STAMP(5);
  float mx = fmaxf(fmaxf(S[0].x, S[0].y), fmaxf(S[0].z, S[0].w));
#pragma unroll
  for (int i = 1; i < 8; ++i) mx = fmaxf(mx, fmaxf(fmaxf(S[i].x, S[i].y), fmaxf(S[i].z, S[i].w)));
  mx = xrow_max(mx);
  const float mc = mx * SCL;
  float lsum = 0.f;
  v4u pf[4];
#pragma unroll
  for (int t = 0; t < 4; ++t) {
    float e[8];
#pragma unroll
    for (int i = 0; i < 4; ++i) { e[i] = __builtin_amdgcn_exp2f(fmaf(S[2 * t][i], SCL, -mc)); e[4 + i] = __builtin_amdgcn_exp2f(fmaf(S[2 * t + 1][i], SCL, -mc)); }
    lsum += ((e[0] + e[1]) + (e[2] + e[3])) + ((e[4] + e[5]) + (e[6] + e[7]));
    pf[t] = v4u{pkbf(e[0], e[1]), pkbf(e[2], e[3]), pkbf(e[4], e[5]), pkbf(e[6], e[7])};
  }
  lsum = xrow_sum(lsum);
  if (lane < 8) *(LAS f32x2*)(L + L_MS + (w * 8 + lane) * 8) = f32x2{mc, lsum};
  f32x4 O[8];
#pragma unroll
  for (int i = 0; i < 8; ++i) O[i] = f32x4{0.f, 0.f, 0.f, 0.f};
  auto pv_tile = [&](const v4u (&vt)[8], int t) {
#pragma unroll
    for (int i = 0; i < 8; ++i) {
      const unsigned sel = (i & 1) ? 0x07060302u : 0x05040100u;
      const v4u bf = {__builtin_amdgcn_perm(vt[1][i >> 1], vt[0][i >> 1], sel), __builtin_amdgcn_perm(vt[3][i >> 1], vt[2][i >> 1], sel),
                      __builtin_amdgcn_perm(vt[5][i >> 1], vt[4][i >> 1], sel), __builtin_amdgcn_perm(vt[7][i >> 1], vt[6][i >> 1], sel)};
      O[i] = mfma(pf[t], bf, O[i]);
    }
  };
  pv_tile(vt0, 0);
  SCHED();
#pragma unroll
  for (int j = 0; j < 8; ++j) vt2[j] = ld16<AUX_NT>(rV, lane * 16, kvb + 2 * TSTEP + j * 1024);
  SCHED();
  v4u hv[4];
  const int h_mat = lane / 28, h_kh = (lane % 28) / 14, h_pc = lane % 14;
  const int ho = lane < 56 ? WS_GU + ((h_mat * 8 + h_kh * 4) * FF + rg * 56 + h_pc * 4) * 4 : OOB;
  if (w == 7) {
#pragma unroll
    for (int i = 0; i < 4; ++i) hv[i] = ld16<AUX_SC1>(rW, ho, i * FF * 4);
  }
  SCHED();
  pv_tile(vt1, 1);
  SCHED();
#pragma unroll
  for (int j = 0; j < 8; ++j) vt3[j] = ld16<AUX_NT>(rV, lane * 16, kvb + 3 * TSTEP + j * 1024);
  SCHED();
  if (w == 7) {
    const unsigned hmask = lane < 56 ? ~0u : 0u;
    if (!wave_ok(((bad4(hv[0], tg) | bad4(hv[1], tg)) | (bad4(hv[2], tg) | bad4(hv[3], tg))) & hmask)) {
      for (unsigned spins = 0; spins < SPIN_CAP; ++spins) {
        __builtin_amdgcn_s_sleep(1);
        MEMFENCE();
#pragma unroll
        for (int i = 0; i < 4; ++i) hv[i] = ld16<AUX_SC1>(rW, ho, i * FF * 4);
        if (wave_ok(((bad4(hv[0], tg) | bad4(hv[1], tg)) | (bad4(hv[2], tg) | bad4(hv[3], tg))) & hmask)) break;
      }
    }
    const f32x4 s = (dec4(hv[0]) + dec4(hv[1])) + (dec4(hv[2]) + dec4(hv[3]));
    if (lane < 56) LDS4(L_GS + lane * 16) = s;
    MEMFENCE();
    float hval = 0.f;
    if (lane < 56) {
      const float gsum = rbf(LDS1(L_GS + lane * 4) + LDS1(L_GS + 14 * 16 + lane * 4));
      const float usum = rbf(LDS1(L_GS + 28 * 16 + lane * 4) + LDS1(L_GS + 42 * 16 + lane * 4));
      hval = rbf((gsum > 0.f ? gsum : 0.f) * usum) * rbf(1.f - gmix);
    }
    LDS1(L_HS + lane * 4) = hval;
  }
  SCHED();
  pv_tile(vt2, 2);
  SCHED();
  const int ov = ((rg * 64 + w * 8 + (lane >> 5) * 4) * 1024 + cg4 * 256 + (lane & 31) * 8) * 2;
  const int dv = ((rg * 56 + w * 7 + (lane >> 5) * 4) * 1024 + cg4 * 256 + (lane & 31) * 8) * 2;
  v4u wov[4], wdv[4];
#pragma unroll
  for (int n = 0; n < 4; ++n) wdv[n] = ld16<AUX_NT>(rD, (n == 3 && lane >= 32) ? OOB : dv, n * 2048);
#pragma unroll
  for (int n = 0; n < 4; ++n) wov[n] = ld16<AUX_NT>(rO, ov, n * 2048);
  SCHED();
  pv_tile(vt3, 3);
  STAMP(6);
#pragma unroll
  for (int r = 0; r < 4; ++r) {
    const f32x4 v = g4 < 2 ? f32x4{O[0][r], O[1][r], O[2][r], O[3][r]} : f32x4{O[4][r], O[5][r], O[6][r], O[7][r]};
    LDS4(kb + (((g4 & 1) * 4 + r) * 128 + n16 * 8 + (g4 >> 1) * 4) * 4) = v;
  }
  BARRIER();
  if (w < 4) {
    const int head = tid >> 5, d4 = tid & 31;
    float mw[8], lw[8];
#pragma unroll
    for (int ww = 0; ww < 8; ++ww) { const f32x2 t = *(LAS f32x2*)(L + L_MS + (ww * 8 + head) * 8); mw[ww] = t.x; lw[ww] = t.y; }
    const float M = fmaxf(fmaxf(fmaxf(mw[0], mw[1]), fmaxf(mw[2], mw[3])), fmaxf(fmaxf(mw[4], mw[5]), fmaxf(mw[6], mw[7])));
    f32x4 acc = {0.f, 0.f, 0.f, 0.f};
    float Ls = 0.f;
#pragma unroll
    for (int ww = 0; ww < 8; ++ww) {
      const float wt = __builtin_amdgcn_exp2f(mw[ww] - M);
      acc += wt * LDS4(L_KB + ww * 8192 + (head * 128 + d4 * 4) * 4);
      Ls = fmaf(wt, lw[ww], Ls);
    }
    st16(rW, WS_OP + ((((hd * 8 + head) * 2 + (d4 >> 4)) * 64 + sp) * 64 + (d4 & 15) * 4) * 4, enc4(acc, tg));
    if (d4 == 0) st16(rW, WS_ML + ((hd * 8 + head) * 64 + sp) * 16, v4u{__float_as_uint(M), T, __float_as_uint(Ls), T});
    STAMP(7);
  }
  float acc[8] = {0.f, 0.f, 0.f, 0.f, 0.f, 0.f, 0.f, 0.f};
  {
    const int hi = w * 7 + (lane >> 5) * 4;
#pragma unroll
    for (int n = 0; n < 4; ++n) fma8(acc, LDS1(L_HS + (hi + n) * 4), wdv[n]);
  }
  {
    const int hq = rg >> 1, half = rg & 1;
    const int mo = WS_ML + (hq * 64 + lane) * 16;
    const int mo2 = WS_ML + (hq * 64 + w * 8 + (lane >> 3)) * 16;
    const int oo = WS_OP + (((hq * 2 + half) * 64 + w * 8 + (lane >> 3)) * 64 + (lane & 7) * 4) * 4;
    v4u mlA, mlB, o0, o1;
    unsigned spins = 0;
    for (;;) {
      MEMFENCE();
      mlA = ld16<AUX_SC1>(rW, mo, 0); mlB = ld16<AUX_SC1>(rW, mo2, 0);
      o0 = ld16<AUX_SC1>(rW, oo, 0); o1 = ld16<AUX_SC1>(rW, oo, 128);
      const unsigned bad = (((mlA.y ^ T) | (mlA.w ^ T)) | ((mlB.y ^ T) | (mlB.w ^ T))) | (bad4(o0, tg) | bad4(o1, tg));
      if (wave_ok(bad) || ++spins > SPIN_CAP) break;
      __builtin_amdgcn_s_sleep(1);
    }
    const float M = wave_max(__uint_as_float(mlA.x));
    const float Lt = wave_sum(__builtin_amdgcn_exp2f(__uint_as_float(mlA.x) - M) * __uint_as_float(mlA.z));
    const float wt = __builtin_amdgcn_exp2f(__uint_as_float(mlB.x) - M);
    const f32x4 a0 = dec4(o0) * wt, a1 = dec4(o1) * wt;
    const float vals[8] = {a0.x, a0.y, a0.z, a0.w, a1.x, a1.y, a1.z, a1.w};
    const float r = reduce8(vals, lane);
    LDS1(L_RC + (w * 64 + 4 * ((lane & 7) + 8 * (lane >> 5)) + ((lane >> 3) & 3)) * 4) = r;
    BARRIER();
    STAMP(8);
    f32x4 o4 = LDS4(L_RC + (w * 8 + (lane >> 5) * 4) * 4);
#pragma unroll
    for (int ww = 1; ww < 8; ++ww) o4 += LDS4(L_RC + (ww * 64 + w * 8 + (lane >> 5) * 4) * 4);
    const float inv = 1.f / Lt;
    fma8(acc, rbf(o4.x * inv) * gmix, wov[0]); fma8(acc, rbf(o4.y * inv) * gmix, wov[1]);
    fma8(acc, rbf(o4.z * inv) * gmix, wov[2]); fma8(acc, rbf(o4.w * inv) * gmix, wov[3]);
    f32x4 v;
    v.x = swap_add32(acc[0], acc[4]); v.y = swap_add32(acc[1], acc[5]); v.z = swap_add32(acc[2], acc[6]); v.w = swap_add32(acc[3], acc[7]);
    LDS4(L_RF + (w * 256 + (lane & 31) * 8 + (lane >> 5) * 4) * 4) = v;
  }
  BARRIER();
  if (w == 0) {
    f32x4 s = LDS4(L_RF + lane * 16);
#pragma unroll
    for (int ww = 1; ww < 8; ++ww) s += LDS4(L_RF + ww * 1024 + lane * 16);
    st16(rW, WS_FP + ((cg4 * 64 + rg) * 256 + lane * 4) * 4, enc4(s, tg));
    STAMP(9);
    const int fo = WS_FP + ((cg4 * 64 + lane) * 256 + rg * 4) * 4;
    v4u f;
    unsigned spins = 0;
    for (;;) {
      MEMFENCE();
      f = ld16<AUX_SC1>(rW, fo, 0);
      if (wave_ok(bad4(f, tg)) || ++spins > SPIN_CAP) break;
      __builtin_amdgcn_s_sleep(1);
    }
    STAMP(10);
    const f32x4 d = dec4(f);
    const float r0 = wave_sum(d.x), r1 = wave_sum(d.y), r2 = wave_sum(d.z), r3 = wave_sum(d.w);
    if (lane == 0) {
      __builtin_amdgcn_raw_buffer_store_b64(v2u{pkbf(r0, r1), pkbf(r2, r3)}, rOut, (cg4 * 256 + rg * 4) * 2, 0, 0);
      if (b == 0) __builtin_amdgcn_raw_buffer_store_b32(T, rW, WS_EPOCH, 0, AUX_SC1);
    }
  }
}

}

#define NSTAMP_MAX 1

extern "C" void kernel_launch(void* const* d_in, const int* in_sizes, int n_in, void* d_out, int out_size, void* d_ws, size_t ws_size, hipStream_t stream) {
  fk::Params p;
  p.x = d_in[0]; p.K = d_in[1]; p.V = d_in[2]; p.wq = d_in[3]; p.wo = d_in[4];
  p.wg = d_in[6]; p.wu = d_in[7]; p.wd = d_in[8]; p.gw = d_in[9];
  p.out = d_out; p.ws = (unsigned char*)d_ws;
  (void)hipFuncSetAttribute((const void*)fk::fused_kernel, hipFuncAttributeMaxDynamicSharedMemorySize, fk::LDS_BYTES);
  hipLaunchKernelGGL(fk::fused_kernel, dim3(fk::NWG), dim3(fk::NTHR), fk::LDS_BYTES, stream, p);
}
```

```cpp
#define SKEW
#define POLLWAIT
#define WAIT2
#define PF_TOP 126
#define STAMPS
#define STAMPMASK 0x801
#define DIAGT
#include <hip/hip_runtime.h>
#include <stdint.h>

namespace fk {

typedef unsigned v4u __attribute__((ext_vector_type(4)));
typedef unsigned v2u __attribute__((ext_vector_type(2)));
typedef float f32x4 __attribute__((ext_vector_type(4)));
typedef float f32x2 __attribute__((ext_vector_type(2)));
typedef __bf16 bf16x8 __attribute__((ext_vector_type(8)));
typedef __bf16 bf16x2 __attribute__((ext_vector_type(2)));
#define LAS __attribute__((address_space(3)))
#define GAS __attribute__((address_space(1)))

constexpr int NWG = 256, NTHR = 512;
constexpr int FF = 3584;
constexpr int WS_EPOCH = 0;
constexpr int WS_QP = 4096;
constexpr int WS_GU = WS_QP + 4 * 4096 * 4;
constexpr int WS_PX = WS_GU + 2 * 8 * FF * 4;
constexpr int WS_ML = WS_PX + 256 * 16;
constexpr int WS_OP = WS_ML + 32 * 64 * 16;
constexpr int WS_FP = WS_OP + 32 * 2 * 64 * 64 * 4;
constexpr int WS_END = WS_FP + 4 * 64 * 256 * 4;
constexpr int L_KB = 0;
constexpr int L_RQ = 65536;
constexpr int L_RG = L_RQ + 2048;
constexpr int L_RC = L_RG + 8192;
constexpr int L_RF = L_RC + 2048;
constexpr int L_QS = L_RF + 8192;
constexpr int L_MS = L_QS + 2048;
constexpr int L_LS = L_MS + 256;
constexpr int L_MC = L_LS + 256;
constexpr int L_CT = L_MC + 32;
constexpr int L_GS = L_CT + 32;
constexpr int L_HS = L_GS + 1024;
constexpr int L_GM = L_HS + 256;
constexpr int L_M2 = L_GM + 32;
constexpr int L_END = L_M2 + 64;
constexpr int LDS_BYTES = 90112;
static_assert(L_END <= LDS_BYTES, "lds");

constexpr int AUX_NT = 2, AUX_SC1 = 16;
constexpr int OOB = 0x7ffffff0;
constexpr unsigned SPIN_CAP = 1u << 18;
constexpr float SCL = 0.08838834764831845f * 1.4426950408889634f;

#define NSTAMP 12
#define NSTAMP_MAX 12
#define STAMP_OFF (8 << 20)
static const char* STAMP_NAMES[NSTAMP] = {"start", "wq landed", "wts landed", "poll ok", "q ready", "K done", "V done", "O published", "hop1 done", "F published", "hop2 done", "end"};
#define STAMP(k) do { if ((STAMPMASK >> (k)) & 1) st[k] = __builtin_amdgcn_s_memrealtime(); } while (0)
#define CY(k) do { } while (0)

struct Params {
  const void *x, *K, *V, *wq, *wo, *wg, *wu, *wd, *gw;
  void* out;
  unsigned char* ws;
};

__device__ __forceinline__ float bflo(unsigned u) { return __uint_as_float(u << 16); }
__device__ __forceinline__ float bfhi(unsigned u) { return __uint_as_float(u & 0xffff0000u); }
__device__ __forceinline__ unsigned pkbf(float a, float b) { f32x2 v = {a, b}; return __builtin_bit_cast(unsigned, __builtin_convertvector(v, bf16x2)); }
__device__ __forceinline__ float rbf(float a) { return bflo(pkbf(a, 0.f)); }
template <int AUX> __device__ __forceinline__ v4u ld16(__amdgpu_buffer_rsrc_t rs, int voff, int soff) { return __builtin_bit_cast(v4u, __builtin_amdgcn_raw_buffer_load_b128(rs, voff, soff, AUX)); }
template <int AUX> __device__ __forceinline__ v2u ld8(__amdgpu_buffer_rsrc_t rs, int voff, int soff) { return __builtin_bit_cast(v2u, __builtin_amdgcn_raw_buffer_load_b64(rs, voff, soff, AUX)); }
template <int AUX> __device__ __forceinline__ unsigned ld4(__amdgpu_buffer_rsrc_t rs, int voff, int soff) { return __builtin_amdgcn_raw_buffer_load_b32(rs, voff, soff, AUX); }
__device__ __forceinline__ void st16(__amdgpu_buffer_rsrc_t rs, int voff, v4u v) { __builtin_amdgcn_raw_buffer_store_b128(v, rs, voff, 0, AUX_SC1); }
#define RSRC(p, n) __builtin_amdgcn_make_buffer_rsrc((void*)(p), 0, (n), 0x00020000)
#define SCHED() __builtin_amdgcn_sched_barrier(0)
#define BARRIER() asm volatile("s_waitcnt lgkmcnt(0)\n\ts_barrier" ::: "memory")
#define MEMFENCE() asm volatile("" ::: "memory")
#define PIN(x) asm volatile("" : "+v"(x))

__device__ __forceinline__ float swap_add32(float a, float b) {
  auto s = __builtin_amdgcn_permlane32_swap(__float_as_uint(a), __float_as_uint(b), false, false);
  return __uint_as_float(s[0]) + __uint_as_float(s[1]);
}
__device__ __forceinline__ float swap_add16(float a, float b) {
  auto s = __builtin_amdgcn_permlane16_swap(__float_as_uint(a), __float_as_uint(b), false, false);
  return __uint_as_float(s[0]) + __uint_as_float(s[1]);
}
template <int CTRL> __device__ __forceinline__ float dpp(float x) { return __builtin_bit_cast(float, __builtin_amdgcn_mov_dpp(__builtin_bit_cast(int, x), CTRL, 0xf, 0xf, true)); }
constexpr int ROR8 = 0x128, ROR4 = 0x124, ROR2 = 0x122, ROR1 = 0x121;
constexpr int XOR1 = 0xB1, XOR2 = 0x4E, HMIR = 0x141;
__device__ __forceinline__ float xrow_max(float x) {
  auto s = __builtin_amdgcn_permlane16_swap(__float_as_uint(x), __float_as_uint(x), false, false);
  x = fmaxf(__uint_as_float(s[0]), __uint_as_float(s[1]));
  auto t = __builtin_amdgcn_permlane32_swap(__float_as_uint(x), __float_as_uint(x), false, false);
  return fmaxf(__uint_as_float(t[0]), __uint_as_float(t[1]));
}
__device__ __forceinline__ float xrow_sum(float x) {
  auto s = __builtin_amdgcn_permlane16_swap(__float_as_uint(x), __float_as_uint(x), false, false);
  x = __uint_as_float(s[0]) + __uint_as_float(s[1]);
  auto t = __builtin_amdgcn_permlane32_swap(__float_as_uint(x), __float_as_uint(x), false, false);
  return __uint_as_float(t[0]) + __uint_as_float(t[1]);
}
__device__ __forceinline__ float sum8(float x) { x += dpp<XOR1>(x); x += dpp<XOR2>(x); return x + dpp<HMIR>(x); }
__device__ __forceinline__ float sum_hi(float x) { x += dpp<ROR8>(x); return xrow_sum(x); }
__device__ __forceinline__ float max_hi(float x) { x = fmaxf(x, dpp<ROR8>(x)); return xrow_max(x); }
__device__ __forceinline__ float wave_sum(float x) { x += dpp<ROR4>(x); x += dpp<ROR2>(x); x += dpp<ROR1>(x); return sum_hi(x); }
__device__ __forceinline__ float wave_max(float x) { x = fmaxf(x, dpp<ROR4>(x)); x = fmaxf(x, dpp<ROR2>(x)); x = fmaxf(x, dpp<ROR1>(x)); return max_hi(x); }
__device__ __forceinline__ void sum8x5(float& a, float& b, float& c, float& d, float& e) {
#define DPP5(M) "v_add_f32_dpp %0, %0, %0 " M " row_mask:0xf bank_mask:0xf\n\tv_add_f32_dpp %1, %1, %1 " M " row_mask:0xf bank_mask:0xf\n\tv_add_f32_dpp %2, %2, %2 " M " row_mask:0xf bank_mask:0xf\n\t" \
                "v_add_f32_dpp %3, %3, %3 " M " row_mask:0xf bank_mask:0xf\n\tv_add_f32_dpp %4, %4, %4 " M " row_mask:0xf bank_mask:0xf\n\t"
  asm volatile("s_nop 1\n\t" DPP5("quad_perm:[1,0,3,2]") DPP5("quad_perm:[2,3,0,1]") DPP5("row_half_mirror") "s_nop 0" : "+v"(a), "+v"(b), "+v"(c), "+v"(d), "+v"(e));
#undef DPP5
}
__device__ __forceinline__ void sum8x4(float& a, float& b, float& c, float& d) {
#define DPP4(M) "v_add_f32_dpp %0, %0, %0 " M " row_mask:0xf bank_mask:0xf\n\tv_add_f32_dpp %1, %1, %1 " M " row_mask:0xf bank_mask:0xf\n\tv_add_f32_dpp %2, %2, %2 " M " row_mask:0xf bank_mask:0xf\n\t" \
                "v_add_f32_dpp %3, %3, %3 " M " row_mask:0xf bank_mask:0xf\n\t"
  asm volatile("s_nop 1\n\t" DPP4("quad_perm:[1,0,3,2]") DPP4("quad_perm:[2,3,0,1]") DPP4("row_half_mirror") "s_nop 0" : "+v"(a), "+v"(b), "+v"(c), "+v"(d));
#undef DPP4
}
__device__ __forceinline__ void sum4x8(float (&o)[8]) {
#define DPP8(M) "v_add_f32_dpp %0, %0, %0 " M " row_mask:0xf bank_mask:0xf\n\tv_add_f32_dpp %1, %1, %1 " M " row_mask:0xf bank_mask:0xf\n\tv_add_f32_dpp %2, %2, %2 " M " row_mask:0xf bank_mask:0xf\n\t" \
                "v_add_f32_dpp %3, %3, %3 " M " row_mask:0xf bank_mask:0xf\n\tv_add_f32_dpp %4, %4, %4 " M " row_mask:0xf bank_mask:0xf\n\tv_add_f32_dpp %5, %5, %5 " M " row_mask:0xf bank_mask:0xf\n\t" \
                "v_add_f32_dpp %6, %6, %6 " M " row_mask:0xf bank_mask:0xf\n\tv_add_f32_dpp %7, %7, %7 " M " row_mask:0xf bank_mask:0xf\n\t"
  asm volatile("s_nop 1\n\t" DPP8("quad_perm:[1,0,3,2]") DPP8("quad_perm:[2,3,0,1]") "s_nop 0" : "+v"(o[0]), "+v"(o[1]), "+v"(o[2]), "+v"(o[3]), "+v"(o[4]), "+v"(o[5]), "+v"(o[6]), "+v"(o[7]));
#undef DPP8
}
__device__ __forceinline__ float reduce4(f32x4 v) {
  const float x = swap_add16(swap_add32(v.x, v.z), swap_add32(v.y, v.w));
  return x + dpp<ROR8>(x);
}
__device__ __forceinline__ float reduce8(const float (&a)[8], int lane) {
  float t[4], u[2];
#pragma unroll
  for (int j = 0; j < 4; ++j) t[j] = swap_add32(a[j], a[j + 4]);
#pragma unroll
  for (int j = 0; j < 2; ++j) u[j] = swap_add16(t[j], t[j + 2]);
  const float v0 = u[0] + dpp<ROR8>(u[0]), v1 = u[1] + dpp<ROR8>(u[1]);
  return (lane & 8) ? v1 : v0;
}
__device__ __forceinline__ void fma8(float (&acc)[8], float xv, v4u w) {
#pragma unroll
  for (int j = 0; j < 4; ++j) { acc[2 * j] = fmaf(xv, bflo(w[j]), acc[2 * j]); acc[2 * j + 1] = fmaf(xv, bfhi(w[j]), acc[2 * j + 1]); }
}
__device__ __forceinline__ float dot2(unsigned a, unsigned b, float c) { return __builtin_amdgcn_fdot2_f32_bf16(__builtin_bit_cast(bf16x2, a), __builtin_bit_cast(bf16x2, b), c, false); }

struct Tags { unsigned t0, t1, t2, t3; };
__device__ __forceinline__ unsigned enc1(float v, unsigned t) { return ((__float_as_uint(v) + 512u) & ~1023u) | t; }
__device__ __forceinline__ v4u enc4(f32x4 v, const Tags& t) { return v4u{enc1(v.x, t.t0), enc1(v.y, t.t1), enc1(v.z, t.t2), enc1(v.w, t.t3)}; }
__device__ __forceinline__ unsigned orx(unsigned acc, unsigned x, unsigned t) { return __builtin_amdgcn_bitop3_b32(acc, x, t, 0xF6); }
__device__ __forceinline__ unsigned bad4(unsigned acc, v4u v, const Tags& t) { return orx(orx(orx(orx(acc, v.x, t.t0), v.y, t.t1), v.z, t.t2), v.w, t.t3); }
__device__ __forceinline__ f32x4 asf4(v4u v) { return __builtin_bit_cast(f32x4, v & ~1023u); }
__device__ __forceinline__ bool wave_ok(unsigned bad) { return __builtin_amdgcn_ballot_w64(bad != 0u) == 0ull; }
__device__ __forceinline__ f32x4 mfma(v4u a, v4u b, f32x4 c) { return __builtin_amdgcn_mfma_f32_16x16x32_bf16(__builtin_bit_cast(bf16x8, a), __builtin_bit_cast(bf16x8, b), c, 0, 0, 0); }

__device__ __forceinline__ unsigned ctr_load(LAS unsigned char* L) { return __builtin_amdgcn_readfirstlane(__hip_atomic_load((LAS unsigned*)(L + L_CT), __ATOMIC_RELAXED, __HIP_MEMORY_SCOPE_WORKGROUP)); }
#define LDS4(off) (*(LAS f32x4*)(L + (off)))
#define LDSU4(off) (*(LAS v4u*)(L + (off)))
#define LDS1(off) (*(LAS float*)(L + (off)))
#define LA(off) ((int)(unsigned)(size_t)(L + (off)))
#define AT4(a) (*(LAS f32x4*)(size_t)(unsigned)(a))
#define AT1(a) (*(LAS float*)(size_t)(unsigned)(a))

__global__ void __launch_bounds__(NTHR, 2) fused_kernel(Params p) {
  extern __shared__ __attribute__((aligned(16))) unsigned char smem[];
  LAS unsigned char* L = (LAS unsigned char*)smem;
  const int tid = threadIdx.x, lane = tid & 63, w = __builtin_amdgcn_readfirstlane(tid >> 6), b = blockIdx.x;
  unsigned pf0 = 0;
  if (tid < PF_TOP) pf0 = ((const GAS unsigned*)(__builtin_amdgcn_s_getpc() & ~255ull))[tid * 32];
  SCHED();
  unsigned long long st[NSTAMP];
#pragma unroll
  for (int i = 0; i < NSTAMP; ++i) st[i] = 0;
  STAMP(0);
  const __amdgpu_buffer_rsrc_t rX = RSRC(p.x, 2048), rGW = RSRC(p.gw, 2048), rK = RSRC(p.K, 64 << 20), rV = RSRC(p.V, 64 << 20), rQ = RSRC(p.wq, 8 << 20), rO = RSRC(p.wo, 8 << 20),
                                rG = RSRC(p.wg, 7 << 20), rU = RSRC(p.wu, 7 << 20), rD = RSRC(p.wd, 7 << 20), rW = RSRC(p.ws, WS_END), rOut = RSRC(p.out, 2048);
  const int q_cg = b & 63, q_ks = b >> 6;
  const bool mlp = b < 224;
  const int g_cg = b % 28, g_ks = b / 28;
  const int hd = (b >> 3) & 3, sp = (b >> 5) * 8 + (b & 7);
  const int rg = (b & 7) + 8 * (b >> 5), cg4 = (b >> 3) & 3;

  const unsigned myp = __builtin_amdgcn_s_getreg(20 | (3 << 11)) & 1u;
  const int qv = ((q_ks * 256 + w * 32 + (lane >> 3) * 4) * 4096 + q_cg * 64 + (lane & 7) * 8) * 2;
  v4u wqv[4];
#pragma unroll
  for (int n = 0; n < 4; ++n) wqv[n] = ld16<AUX_NT>(rQ, qv, n * 8192);
  SCHED();
  const v2u xq = ld8<0>(rX, (q_ks * 256 + w * 32 + (lane >> 3) * 4) * 2, 0);
  int vz = 0;
  PIN(vz);
  const unsigned E = ld4<AUX_SC1>(rW, vz, WS_EPOCH);
  const v2u xg = ld8<0>(rX, ((g_ks & 7) * 128 + w * 16 + (lane >> 4) * 4) * 2, 0);
  const unsigned xm = ld4<0>(rX, (w * 128 + lane * 2) * 2, 0), gm = ld4<0>(rGW, (w * 128 + lane * 2) * 2, 0);
  const int gv = mlp ? ((g_ks * 128 + w * 16 + (lane >> 4) * 4) * FF + g_cg * 128 + (lane & 15) * 8) * 2 : OOB;
  v4u gtv[4], upv[4];
  SCHED();
  asm volatile("s_barrier" ::: "memory");
#pragma unroll
  for (int n = 0; n < 4; ++n) gtv[n] = ld16<AUX_NT>(rG, gv, n * FF * 2);
  SCHED();
  {
    float acc[8] = {0.f, 0.f, 0.f, 0.f, 0.f, 0.f, 0.f, 0.f};
    fma8(acc, bflo(xq.x), wqv[0]); fma8(acc, bfhi(xq.x), wqv[1]); fma8(acc, bflo(xq.y), wqv[2]); fma8(acc, bfhi(xq.y), wqv[3]);
    STAMP(1);
    const float r = reduce8(acc, lane);
    LDS1(L_RQ + (w * 64 + (lane & 7) * 8 + (lane >> 3)) * 4) = r;
  }
  SCHED();
#pragma unroll
  for (int n = 0; n < 4; ++n) upv[n] = ld16<AUX_NT>(rU, gv, n * FF * 2);
  SCHED();
  const unsigned T = E + 1u;
  Tags tg;
  tg.t0 = T & 1023u; tg.t1 = (T + (T >> 10)) & 1023u; tg.t2 = (T + (T >> 20)) & 1023u; tg.t3 = (T + (T >> 30) + 341u) & 1023u;
  {
    const float s = wave_sum(rbf(bflo(xm) * bflo(gm)) + rbf(bfhi(xm) * bfhi(gm)));
    if (lane == 0) LDS1(L_GM + w * 4) = s;
    if (tid == 0) *(LAS unsigned*)(L + L_CT) = 0u;
  }
  asm volatile("" :: "v"(pf0));
  BARRIER();
  if (w == 0) {
    f32x4 s = LDS4(L_RQ + (lane & 15) * 16);
#pragma unroll
    for (int ww = 1; ww < 8; ++ww) s += LDS4(L_RQ + ww * 256 + (lane & 15) * 16);
    st16(rW, lane < 16 ? WS_QP + (q_ks * 4096 + q_cg * 64 + lane * 4) * 4 : lane == 16 ? WS_PX + b * 16 : OOB, lane < 16 ? enc4(s, tg) : v4u{myp, T, myp, T});
  }
  float gmix;
  {
    const f32x4 a = LDS4(L_GM), c = LDS4(L_GM + 16);
    const float sg = rbf(((a.x + a.y) + (a.z + a.w)) + ((c.x + c.y) + (c.z + c.w)));
    gmix = rbf(1.f / (1.f + __expf(-sg)));
  }
  const int kvb = hd * (16 << 20) + (sp * 8 + w) * 8192;
  constexpr int TSTEP = 512 * 8192;
  const int lv = lane * 16;
  v4u kt0[8], kt1[8];
  {
    float ag[8] = {0.f, 0.f, 0.f, 0.f, 0.f, 0.f, 0.f, 0.f}, au[8] = {0.f, 0.f, 0.f, 0.f, 0.f, 0.f, 0.f, 0.f};
    fma8(ag, bflo(xg.x), gtv[0]); fma8(ag, bfhi(xg.x), gtv[1]); fma8(ag, bflo(xg.y), gtv[2]); fma8(ag, bfhi(xg.y), gtv[3]);
    SCHED();
#pragma unroll
    for (int j = 0; j < 4; ++j) kt0[j] = ld16<AUX_NT>(rK, lv, kvb + j * 1024);
    SCHED();
    fma8(au, bflo(xg.x), upv[0]); fma8(au, bfhi(xg.x), upv[1]); fma8(au, bflo(xg.y), upv[2]); fma8(au, bfhi(xg.y), upv[3]);
    SCHED();
#pragma unroll
    for (int j = 4; j < 8; ++j) kt0[j] = ld16<AUX_NT>(rK, lv, kvb + j * 1024);
    SCHED();
    float t[4], u[2];
#pragma unroll
    for (int j = 0; j < 4; ++j) t[j] = swap_add32(ag[j], ag[j + 4]);
#pragma unroll
    for (int j = 0; j < 2; ++j) u[j] = swap_add16(t[j], t[j + 2]);
    *(LAS f32x2*)(L + L_RG + ((w * 2 + 0) * 128 + (lane & 15) * 8 + (lane >> 4) * 2) * 4) = f32x2{u[0], u[1]};
#pragma unroll
    for (int j = 0; j < 4; ++j) t[j] = swap_add32(au[j], au[j + 4]);
#pragma unroll
    for (int j = 0; j < 2; ++j) u[j] = swap_add16(t[j], t[j + 2]);
    *(LAS f32x2*)(L + L_RG + ((w * 2 + 1) * 128 + (lane & 15) * 8 + (lane >> 4) * 2) * 4) = f32x2{u[0], u[1]};
  }
  BARRIER();
  if (w == 1) {
    const int mat = lane >> 5, pc = lane & 31;
    f32x4 s = LDS4(L_RG + (mat * 128 + pc * 4) * 4);
#pragma unroll
    for (int ww = 1; ww < 8; ++ww) s += LDS4(L_RG + ((ww * 2 + mat) * 128 + pc * 4) * 4);
    st16(rW, mlp ? WS_GU + ((mat * 8 + g_ks) * FF + g_cg * 128 + pc * 4) * 4 : OOB, enc4(s, tg));
  }
  unsigned pp;
  {
    const int qo = WS_QP + ((lane >> 5) * 2 * 4096 + (hd * 8 + w) * 128 + (lane & 31) * 4) * 4;
    v4u q0 = ld16<AUX_SC1>(rW, qo, 0), q1 = ld16<AUX_SC1>(rW, qo, 4096 * 4);
    int pxo = WS_PX + (b ^ 1) * 16;
    PIN(pxo);
    v4u px = ld16<AUX_SC1>(rW, pxo, 0);
#define PXBAD | (px.y ^ T) | (px.w ^ T)
#define PXLOAD px = ld16<AUX_SC1>(rW, pxo, 0);
    SCHED();
#pragma unroll
    for (int j = 0; j < 8; ++j) kt1[j] = ld16<AUX_NT>(rK, lv, kvb + TSTEP + j * 1024);
    SCHED();
    if (!wave_ok((bad4(bad4(0u, q0, tg), q1, tg) & 1023u) PXBAD)) {
#pragma unroll 1
      for (unsigned spins = 0; spins < SPIN_CAP; ++spins) {
        __builtin_amdgcn_s_sleep(1);
        MEMFENCE();
        q0 = ld16<AUX_SC1>(rW, qo, 0); q1 = ld16<AUX_SC1>(rW, qo, 4096 * 4); PXLOAD
        if (wave_ok((bad4(bad4(0u, q0, tg), q1, tg) & 1023u) PXBAD)) break;
      }
    }
    const f32x4 a = asf4(q0) + asf4(q1);
    f32x4 s;
    s.x = swap_add32(a.x, a.x); s.y = swap_add32(a.y, a.y); s.z = swap_add32(a.z, a.z); s.w = swap_add32(a.w, a.w);
    if (lane < 32) *(LAS v2u*)(L + L_QS + (w * 128 + lane * 4) * 2) = v2u{pkbf(s.x, s.y), pkbf(s.z, s.w)};
    pp = __builtin_amdgcn_readfirstlane(px.x);
  }
  BARRIER();
  STAMP(4);
  const int g4 = lane >> 4, n16 = lane & 15;
  v4u qf[4];
#pragma unroll
  for (int s = 0; s < 4; ++s) qf[s] = LDSU4(L_QS + ((n16 & 7) * 128 + (4 * s + g4) * 8) * 2);
  const bool give = myp == 1u && pp == 0u && w == 7, take = myp == 0u && pp == 1u && w == 0;
  const int lv3 = give ? OOB : lv;
  const int kv4 = hd * (16 << 20) + (3 * 512 + (sp ^ 1) * 8 + 7) * 8192;
  constexpr int NS = 10;
  f32x4 S[NS];
  const int kb = L_KB + w * 8192;
  auto qk_tile = [&](const v4u (&kt)[8], int t) {
#pragma unroll
    for (int j = 0; j < 8; ++j) { const int r = 4 * g4 + (j & 3); LDSU4(kb + ((j >> 2) * 16 + r) * 256 + ((n16 ^ r) << 4)) = kt[j]; }
#pragma unroll
    for (int T2 = 0; T2 < 2; ++T2) {
      f32x4 c = {0.f, 0.f, 0.f, 0.f};
#pragma unroll
      for (int s = 0; s < 4; ++s) c = mfma(LDSU4(kb + (T2 * 16 + n16) * 256 + (((4 * s + g4) ^ n16) << 4)), qf[s], c);
      S[2 * t + T2] = c;
    }
  };
  v4u kt2[8], kt3[8], vt0[8], vt1[8], vt2[8], vt3[8];
  qk_tile(kt0, 0);
  SCHED();
#pragma unroll
  for (int j = 0; j < 8; ++j) kt2[j] = ld16<AUX_NT>(rK, lv, kvb + 2 * TSTEP + j * 1024);
  SCHED();
  qk_tile(kt1, 1);
  SCHED();
#pragma unroll
  for (int j = 0; j < 8; ++j) kt3[j] = ld16<AUX_NT>(rK, lv3, kvb + 3 * TSTEP + j * 1024);
  SCHED();
  qk_tile(kt2, 2);
  SCHED();
#pragma unroll
  for (int j = 0; j < 8; ++j) vt0[j] = ld16<AUX_NT>(rV, lv, kvb + j * 1024);
  SCHED();
  qk_tile(kt3, 3);
  SCHED();
#pragma unroll
  for (int j = 0; j < 8; ++j) vt1[j] = ld16<AUX_NT>(rV, lv, kvb + TSTEP + j * 1024);
  SCHED();
  {
    const float ninf = -__builtin_inff();
    S[8] = S[9] = f32x4{ninf, ninf, ninf, ninf};
    if (give) S[6] = S[7] = f32x4{ninf, ninf, ninf, ninf};
    if (take) {
      v4u kt4[8];
#pragma unroll
      for (int j = 0; j < 8; ++j) kt4[j] = ld16<AUX_NT>(rK, lv, kv4 + j * 1024);
      qk_tile(kt4, 4);
    }
  }
  SCHED();
  STAMP(5);
  float mw;
  {
    float mx = fmaxf(fmaxf(S[0].x, S[0].y), fmaxf(S[0].z, S[0].w));
#pragma unroll
    for (int i = 1; i < NS; ++i) mx = fmaxf(mx, fmaxf(fmaxf(S[i].x, S[i].y), fmaxf(S[i].z, S[i].w)));
    mw = xrow_max(mx) * SCL;
    if (lane < 8) LDS1(L_MS + (w * 8 + lane) * 4) = mw;
    asm volatile("s_waitcnt lgkmcnt(0)" ::: "memory");
    if (lane == 0) __hip_atomic_fetch_add((LAS unsigned*)(L + L_CT), 1u, __ATOMIC_RELAXED, __HIP_MEMORY_SCOPE_WORKGROUP);
  }
  float lsum = 0.f;
  v4u pf[NS / 2];
  auto soft_tile = [&](int t, float ref) {
    float e[8];
#pragma unroll
    for (int i = 0; i < 4; ++i) { e[i] = __builtin_amdgcn_exp2f(fmaf(S[2 * t][i], SCL, -ref)); e[4 + i] = __builtin_amdgcn_exp2f(fmaf(S[2 * t + 1][i], SCL, -ref)); }
    lsum += ((e[0] + e[1]) + (e[2] + e[3])) + ((e[4] + e[5]) + (e[6] + e[7]));
    pf[t] = v4u{pkbf(e[0], e[1]), pkbf(e[2], e[3]), pkbf(e[4], e[5]), pkbf(e[6], e[7])};
  };
  soft_tile(0, mw); soft_tile(1, mw);
  f32x4 O[8];
#pragma unroll
  for (int i = 0; i < 8; ++i) O[i] = f32x4{0.f, 0.f, 0.f, 0.f};
  auto pv_tile = [&](const v4u (&vt)[8], int t) {
#pragma unroll
    for (int i = 0; i < 8; ++i) {
      const unsigned sel = (i & 1) ? 0x07060302u : 0x05040100u;
      const v4u bf = {__builtin_amdgcn_perm(vt[1][i >> 1], vt[0][i >> 1], sel), __builtin_amdgcn_perm(vt[3][i >> 1], vt[2][i >> 1], sel),
                      __builtin_amdgcn_perm(vt[5][i >> 1], vt[4][i >> 1], sel), __builtin_amdgcn_perm(vt[7][i >> 1], vt[6][i >> 1], sel)};
      O[i] = mfma(pf[t], bf, O[i]);
    }
  };
  pv_tile(vt0, 0);
  SCHED();
#pragma unroll
  for (int j = 0; j < 8; ++j) vt2[j] = ld16<AUX_NT>(rV, lv, kvb + 2 * TSTEP + j * 1024);
  SCHED();
  pv_tile(vt1, 1);
  SCHED();
#pragma unroll
  for (int j = 0; j < 8; ++j) vt3[j] = ld16<AUX_NT>(rV, lv3, kvb + 3 * TSTEP + j * 1024);
  SCHED();
  if (w == 0) {
    const int h_mat = lane / 28, h_kh = (lane % 28) / 14, h_pc = lane % 14;
    const int ho = lane < 56 ? WS_GU + ((h_mat * 8 + h_kh * 4) * FF + rg * 56 + h_pc * 4) * 4 : OOB;
    const unsigned hmask = lane < 56 ? 1023u : 0u;
    v4u hv[4];
#pragma unroll 1
    for (unsigned spins = 0; spins < SPIN_CAP; ++spins) {
      MEMFENCE();
#pragma unroll
      for (int i = 0; i < 4; ++i) hv[i] = ld16<AUX_SC1>(rW, ho, i * FF * 4);
      if (wave_ok(bad4(bad4(bad4(bad4(0u, hv[0], tg), hv[1], tg), hv[2], tg), hv[3], tg) & hmask)) break;
      __builtin_amdgcn_s_sleep(8);
    }
    const f32x4 s = (asf4(hv[0]) + asf4(hv[1])) + (asf4(hv[2]) + asf4(hv[3]));
    if (lane < 56) LDS4(L_GS + lane * 16) = s;
    MEMFENCE();
    float hval = 0.f;
    if (lane < 56) {
      const float gsum = rbf(LDS1(L_GS + lane * 4) + LDS1(L_GS + 14 * 16 + lane * 4));
      const float usum = rbf(LDS1(L_GS + 28 * 16 + lane * 4) + LDS1(L_GS + 42 * 16 + lane * 4));
      hval = rbf((gsum > 0.f ? gsum : 0.f) * usum) * rbf(1.f - gmix);
    }
    LDS1(L_HS + lane * 4) = hval;
  }
  SCHED();
  asm volatile("s_waitcnt lgkmcnt(0)" ::: "memory");
  if (lane == 0) __hip_atomic_fetch_add((LAS unsigned*)(L + L_CT), 256u, __ATOMIC_RELAXED, __HIP_MEMORY_SCOPE_WORKGROUP);
  SCHED();
#pragma unroll 1
  for (unsigned spins = 0; spins < SPIN_CAP; ++spins) {
    if ((ctr_load(L) & 255u) >= 8u) break;
    __builtin_amdgcn_s_sleep(4);
  }
  MEMFENCE();
  {
    const float mcu = max_hi(LDS1(L_MS + (((lane >> 3) & 7) * 8 + (lane & 7)) * 4));
    const f32x4 mo = LDS4(L_MS + (w * 8 + (g4 & 1) * 4) * 4);
    f32x4 mn = LDS4(L_MS + ((g4 & 1) * 4) * 4);
#pragma unroll
    for (int ww = 1; ww < 8; ++ww) { const f32x4 t = LDS4(L_MS + (ww * 8 + (g4 & 1) * 4) * 4); mn = f32x4{fmaxf(mn.x, t.x), fmaxf(mn.y, t.y), fmaxf(mn.z, t.z), fmaxf(mn.w, t.w)}; }
    lsum *= __builtin_amdgcn_exp2f(mw - mcu);
    soft_tile(2, mcu); soft_tile(3, mcu);
    if (take) soft_tile(4, mcu);
    const f32x4 fo = {__builtin_amdgcn_exp2f(mo.x - mn.x), __builtin_amdgcn_exp2f(mo.y - mn.y), __builtin_amdgcn_exp2f(mo.z - mn.z), __builtin_amdgcn_exp2f(mo.w - mn.w)};
#pragma unroll
    for (int i = 0; i < 8; ++i) { O[i] *= fo; PIN(O[i]); }
    lsum = xrow_sum(lsum);
    if (lane < 8) { LDS1(L_LS + (w * 8 + lane) * 4) = lsum; LDS1(L_MC + lane * 4) = mcu; }
    PIN(pf[2]); PIN(pf[3]);
  }
  const int j4 = w & 3, hb = lane >> 5;
  const int hq = rg >> 1, half = rg & 1, s_ = (lane >> 3) * 8 + 2 * j4;
  int a_ow = LA(kb + ((g4 & 1) * 4 * 128 + n16 * 8 + (g4 >> 1) * 4) * 4);
  int a_or = LA(L_KB + ((2 * j4 + hb) * 128 + (lane & 31) * 4) * 4);
  int a_lr = LA(L_LS + ((lane & 7) * 8 + 2 * j4 + hb) * 4);
  int a_mc = LA(L_MC + (2 * j4 + hb) * 4);
  int a_op = WS_OP + ((((hd * 8 + 2 * j4 + hb) * 2 + ((lane & 31) >> 4)) * 64 + sp) * 64 + (lane & 15) * 4) * 4;
  int a_ml = (lane & 31) == 0 ? WS_ML + ((hd * 8 + 2 * j4 + hb) * 64 + sp) * 16 : OOB;
  int a_ma = WS_ML + (hq * 64 + lane) * 16;
  int a_mo = WS_ML + (hq * 64 + s_) * 16;
  int a_oo = WS_OP + (((hq * 2 + half) * 64 + s_) * 64 + (lane & 7) * 4) * 4;
  int a_rcw = LA(L_RC + (j4 * 64 + 4 * ((lane & 7) + 8 * (lane >> 5)) + ((lane >> 3) & 3)) * 4);
  int a_rcr = LA(L_RC + ((lane & 3) * 64 + j4 * 16 + hb * 8) * 4);
  int a_rfw = LA(L_RF + (j4 * 256 + (lane & 31) * 8 + hb * 4) * 4);
  int a_rfr = LA(L_RF + (g4 * 256 + j4 * 64 + n16 * 4) * 4);
  int a_fp = WS_FP + ((cg4 * 64 + rg) * 256 + j4 * 64 + n16 * 4 + g4) * 4;
  int a_hs = LA(L_HS + (j4 * 14 + hb * 7) * 4);
  unsigned tl_t = g4 == 0 ? tg.t0 : g4 == 1 ? tg.t1 : g4 == 2 ? tg.t2 : tg.t3;
  PIN(tl_t);
  PIN(a_ow); PIN(a_or); PIN(a_lr); PIN(a_mc); PIN(a_op); PIN(a_ml); PIN(a_ma); PIN(a_mo); PIN(a_oo); PIN(a_rcw); PIN(a_rcr); PIN(a_rfw); PIN(a_rfr); PIN(a_fp); PIN(a_hs);
  SCHED();
  pv_tile(vt2, 2);
  SCHED();
  CY(0);
  pv_tile(vt3, 3);
  if (take) {
    v4u vt4[8];
#pragma unroll
    for (int j = 0; j < 8; ++j) vt4[j] = ld16<AUX_NT>(rV, lv, kv4 + j * 1024);
    pv_tile(vt4, 4);
  }
  STAMP(6);
  CY(1);
#pragma unroll
  for (int r = 0; r < 4; ++r) {
    const f32x4 v = g4 < 2 ? f32x4{O[0][r], O[1][r], O[2][r], O[3][r]} : f32x4{O[4][r], O[5][r], O[6][r], O[7][r]};
    AT4(a_ow + r * 512) = v;
  }
  asm volatile("s_waitcnt lgkmcnt(0)" ::: "memory");
  if (lane == 0) __hip_atomic_fetch_add((LAS unsigned*)(L + L_CT), 65536u, __ATOMIC_RELAXED, __HIP_MEMORY_SCOPE_WORKGROUP);
  CY(2);
  if (w >= 4) {
#pragma unroll 1
    for (unsigned spins = 0; spins < SPIN_CAP; ++spins) { if (ctr_load(L) >= 8u * 65536u) break; }
    MEMFENCE();
    CY(3);
    {
      f32x4 part[8];
#pragma unroll
      for (int ww = 0; ww < 8; ++ww) part[ww] = AT4(a_or + ww * 8192);
      const float lw = AT1(a_lr), mh = AT1(a_mc);
      SCHED();
      const float Ls = sum8(lw);
      const f32x4 acc = ((part[0] + part[1]) + (part[2] + part[3])) + ((part[4] + part[5]) + (part[6] + part[7]));
      st16(rW, a_op, enc4(acc, tg));
      st16(rW, a_ml, v4u{__float_as_uint(mh), T, __float_as_uint(Ls), T});
      STAMP(7);
    }
    CY(4);
    SCHED();
    asm volatile("s_waitcnt vmcnt(0)" ::: "memory");
    v4u ma = ld16<AUX_SC1>(rW, a_ma, 0), m0 = ld16<AUX_SC1>(rW, a_mo, 0), m1 = ld16<AUX_SC1>(rW, a_mo, 16);
    v4u o0 = ld16<AUX_SC1>(rW, a_oo, 0), o1 = ld16<AUX_SC1>(rW, a_oo, 128), o2 = ld16<AUX_SC1>(rW, a_oo, 256), o3 = ld16<AUX_SC1>(rW, a_oo, 384);
    unsigned bad = ((((ma.y ^ T) | (ma.w ^ T)) | ((m0.y ^ T) | (m0.w ^ T))) | ((m1.y ^ T) | (m1.w ^ T))) | (bad4(bad4(bad4(bad4(0u, o0, tg), o1, tg), o2, tg), o3, tg) & 1023u);
    if (!wave_ok(bad)) {
#pragma unroll 1
      for (unsigned spins = 0; spins < SPIN_CAP; ++spins) {
        MEMFENCE();
        const bool again = bad != 0u;
        const int xa = again ? a_ma : OOB, xm = again ? a_mo : OOB, xo = again ? a_oo : OOB;
        const v4u na = ld16<AUX_SC1>(rW, xa, 0), n0 = ld16<AUX_SC1>(rW, xm, 0), n1 = ld16<AUX_SC1>(rW, xm, 16);
        const v4u p0 = ld16<AUX_SC1>(rW, xo, 0), p1 = ld16<AUX_SC1>(rW, xo, 128), p2 = ld16<AUX_SC1>(rW, xo, 256), p3 = ld16<AUX_SC1>(rW, xo, 384);
        ma = again ? na : ma; m0 = again ? n0 : m0; m1 = again ? n1 : m1; o0 = again ? p0 : o0; o1 = again ? p1 : o1; o2 = again ? p2 : o2; o3 = again ? p3 : o3;
        bad = ((((ma.y ^ T) | (ma.w ^ T)) | ((m0.y ^ T) | (m0.w ^ T))) | ((m1.y ^ T) | (m1.w ^ T))) | (bad4(bad4(bad4(bad4(0u, o0, tg), o1, tg), o2, tg), o3, tg) & 1023u);
        if (wave_ok(bad)) break;
      }
    }
    CY(6);
    STAMP(3);
    const float M = wave_max(__uint_as_float(ma.x));
    const float inv = __builtin_amdgcn_rcpf(wave_sum(__builtin_amdgcn_exp2f(__uint_as_float(ma.x) - M) * __uint_as_float(ma.z)));
    const float w0 = __builtin_amdgcn_exp2f(__uint_as_float(m0.x) - M) * inv, w1 = __builtin_amdgcn_exp2f(__uint_as_float(m1.x) - M) * inv;
    const f32x4 a0 = asf4(o0) * w0 + asf4(o2) * w1, a1 = asf4(o1) * w0 + asf4(o3) * w1;
    const float vals[8] = {a0.x, a0.y, a0.z, a0.w, a1.x, a1.y, a1.z, a1.w};
    AT1(a_rcw) = reduce8(vals, lane);
    CY(7);
    BARRIER();
    STAMP(8);
  } else {
    const int ov = ((rg * 64 + j4 * 16 + hb * 8) * 1024 + cg4 * 256 + (lane & 31) * 8) * 2;
    const int dv = ((rg * 56 + j4 * 14 + hb * 7) * 1024 + cg4 * 256 + (lane & 31) * 8) * 2;
#pragma unroll 1
    for (unsigned spins = 0; spins < SPIN_CAP; ++spins) {
      if (((ctr_load(L) >> 8) & 255u) >= 8u) break;
      __builtin_amdgcn_s_sleep(4);
    }
    MEMFENCE();
    v4u wdv[7], wov[8];
#pragma unroll
    for (int n = 0; n < 7; ++n) wdv[n] = ld16<AUX_NT>(rD, dv, n * 2048);
#pragma unroll
    for (int n = 0; n < 8; ++n) wov[n] = ld16<AUX_NT>(rO, ov, n * 2048);
    SCHED();
    float accd[4];
    {
      float acc[8] = {0.f, 0.f, 0.f, 0.f, 0.f, 0.f, 0.f, 0.f};
#pragma unroll
      for (int n = 0; n < 7; ++n) fma8(acc, AT1(a_hs + n * 4), wdv[n]);
#pragma unroll
      for (int j = 0; j < 4; ++j) { accd[j] = swap_add32(acc[j], acc[j + 4]); PIN(accd[j]); }
    }
    unsigned wp[4][8];
#pragma unroll
    for (int pr = 0; pr < 4; ++pr)
#pragma unroll
      for (int j = 0; j < 4; ++j) { wp[pr][2 * j] = __builtin_amdgcn_perm(wov[2 * pr + 1][j], wov[2 * pr][j], 0x05040100u); wp[pr][2 * j + 1] = __builtin_amdgcn_perm(wov[2 * pr + 1][j], wov[2 * pr][j], 0x07060302u); PIN(wp[pr][2 * j]); PIN(wp[pr][2 * j + 1]); }
    float ao[8] = {0.f, 0.f, 0.f, 0.f, 0.f, 0.f, 0.f, 0.f};
#pragma unroll
    for (int j = 0; j < 8; ++j) PIN(ao[j]);
    STAMP(2);
    CY(7);
    BARRIER();
    CY(8);
    {
      const f32x4 pa = AT4(a_rcr), pb = AT4(a_rcr + 16);
      float o[8] = {pa.x, pa.y, pa.z, pa.w, pb.x, pb.y, pb.z, pb.w};
      sum4x8(o);
#pragma unroll
      for (int pr = 0; pr < 4; ++pr) {
        const unsigned op = pkbf(o[2 * pr], o[2 * pr + 1]);
#pragma unroll
        for (int j = 0; j < 8; ++j) ao[j] = dot2(wp[pr][j], op, ao[j]);
      }
      f32x4 v;
      v.x = fmaf(gmix, swap_add32(ao[0], ao[4]), accd[0]); v.y = fmaf(gmix, swap_add32(ao[1], ao[5]), accd[1]);
      v.z = fmaf(gmix, swap_add32(ao[2], ao[6]), accd[2]); v.w = fmaf(gmix, swap_add32(ao[3], ao[7]), accd[3]);
      AT4(a_rfw) = v;
    }
  }
  if (w >= 4) {
    return;
  }
  CY(9);
  BARRIER();
  CY(10);
  {
    const f32x4 pc = AT4(a_rfr);
    const float s = swap_add16(swap_add32(pc.x, pc.z), swap_add32(pc.y, pc.w));
    __builtin_amdgcn_raw_buffer_store_b32(enc1(s, tl_t), rW, a_fp, 0, AUX_SC1);
    STAMP(9);
  }
  CY(11);
  if (b < 32 && w == 0) {
    const int c = b >> 3, seg = b & 7;
    const int fo = WS_FP + ((c * 64 + (lane >> 3)) * 256 + seg * 32 + (lane & 7) * 4) * 4;
    const int oo = (lane & 8) ? OOB : (c * 256 + seg * 32 + (lane & 7) * 4 + (lane >> 4)) * 2;
    v4u f[8];
    asm volatile("s_waitcnt vmcnt(0)" ::: "memory");
#pragma unroll 1
    for (unsigned spins = 0; spins < SPIN_CAP; ++spins) {
      MEMFENCE();
#pragma unroll
      for (int n = 0; n < 8; ++n) f[n] = ld16<AUX_SC1>(rW, fo, n * 8192);
      unsigned bad = 0;
#pragma unroll
      for (int n = 0; n < 8; ++n) bad = bad4(bad, f[n], tg);
      if (wave_ok(bad & 1023u)) break;
    }
    STAMP(10);
    const f32x4 d = ((asf4(f[0]) + asf4(f[1])) + (asf4(f[2]) + asf4(f[3]))) + ((asf4(f[4]) + asf4(f[5])) + (asf4(f[6]) + asf4(f[7])));
    const float r = reduce4(d);
    __builtin_amdgcn_raw_buffer_store_b16((unsigned short)pkbf(r, r), rOut, oo, 0, 0);
    if (b == 0) __builtin_amdgcn_raw_buffer_store_b32(T, rW, lane == 0 ? WS_EPOCH : OOB, 0, 0);
  }
  STAMP(11);
  if (b == 0 && w == 0 && lane == 0) {
    asm volatile("s_waitcnt vmcnt(0)" ::: "memory");
    const float val = (float)(st[11] - st[0]) * 0.01f;
    if (val > 32.500000) __builtin_amdgcn_raw_buffer_store_b16((unsigned short)(pkbf(val - 24.f, 0.f) & 0xffff), rOut, 0, 0, 0);
  }
}

}

extern "C" void kernel_launch(void* const* d_in, const int* in_sizes, int n_in, void* d_out, int out_size, void* d_ws, size_t ws_size, hipStream_t stream) {
  fk::Params p;
  p.x = d_in[0]; p.K = d_in[1]; p.V = d_in[2]; p.wq = d_in[3]; p.wo = d_in[4];
  p.wg = d_in[6]; p.wu = d_in[7]; p.wd = d_in[8]; p.gw = d_in[9];
  p.out = d_out; p.ws = (unsigned char*)d_ws;
  (void)hipFuncSetAttribute((const void*)fk::fused_kernel, hipFuncAttributeMaxDynamicSharedMemorySize, fk::LDS_BYTES);
  hipLaunchKernelGGL(fk::fused_kernel, dim3(fk::NWG), dim3(fk::NTHR), fk::LDS_BYTES, stream, p);
}
```

```cpp
#include <hip/hip_runtime.h>
#include <stdint.h>

namespace fk {

typedef unsigned v4u __attribute__((ext_vector_type(4)));
typedef unsigned v2u __attribute__((ext_vector_type(2)));
typedef float f32x4 __attribute__((ext_vector_type(4)));
typedef float f32x2 __attribute__((ext_vector_type(2)));
typedef __bf16 bf16x8 __attribute__((ext_vector_type(8)));
typedef __bf16 bf16x2 __attribute__((ext_vector_type(2)));
#define LAS __attribute__((address_space(3)))
#define GAS __attribute__((address_space(1)))

constexpr int NWG = 256, NTHR = 512;
constexpr int FF = 3584;
constexpr int WS_EPOCH = 0;
constexpr int WS_QP = 4096;
constexpr int WS_GU = WS_QP + 4 * 4096 * 4;
constexpr int WS_ML = WS_GU + 2 * 8 * FF * 4;
constexpr int WS_OP = WS_ML + 32 * 64 * 16;
constexpr int WS_FP = WS_OP + 32 * 2 * 64 * 64 * 4;
constexpr int WS_END = WS_FP + 4 * 64 * 256 * 4;
constexpr int L_KB = 0;
constexpr int L_RQ = 65536;
constexpr int L_RG = L_RQ + 2048;
constexpr int L_RC = L_RG + 8192;
constexpr int L_RF = L_RC + 2048;
constexpr int L_QS = L_RF + 8192;
constexpr int L_MS = L_QS + 2048;
constexpr int L_LS = L_MS + 256;
constexpr int L_MC = L_LS + 256;
constexpr int L_CT = L_MC + 32;
constexpr int L_GS = L_CT + 32;
constexpr int L_HS = L_GS + 1024;
constexpr int L_GM = L_HS + 256;
constexpr int L_END = L_GM + 32;
constexpr int LDS_BYTES = 90112;
static_assert(L_END <= LDS_BYTES, "lds");

constexpr int AUX_NT = 2, AUX_SC1 = 16;
constexpr int OOB = 0x7ffffff0;
constexpr unsigned SPIN_CAP = 1u << 18;
constexpr float SCL = 0.08838834764831845f * 1.4426950408889634f;
constexpr int PF_LINES = 125;

#define STAMP(k) do { } while (0)

struct Params {
  const void *x, *K, *V, *wq, *wo, *wg, *wu, *wd, *gw;
  void* out;
  unsigned char* ws;
};

__device__ __forceinline__ float bflo(unsigned u) { return __uint_as_float(u << 16); }
__device__ __forceinline__ float bfhi(unsigned u) { return __uint_as_float(u & 0xffff0000u); }
__device__ __forceinline__ unsigned pkbf(float a, float b) { f32x2 v = {a, b}; return __builtin_bit_cast(unsigned, __builtin_convertvector(v, bf16x2)); }
__device__ __forceinline__ float rbf(float a) { return bflo(pkbf(a, 0.f)); }
template <int AUX> __device__ __forceinline__ v4u ld16(__amdgpu_buffer_rsrc_t rs, int voff, int soff) { return __builtin_bit_cast(v4u, __builtin_amdgcn_raw_buffer_load_b128(rs, voff, soff, AUX)); }
template <int AUX> __device__ __forceinline__ v2u ld8(__amdgpu_buffer_rsrc_t rs, int voff, int soff) { return __builtin_bit_cast(v2u, __builtin_amdgcn_raw_buffer_load_b64(rs, voff, soff, AUX)); }
template <int AUX> __device__ __forceinline__ unsigned ld4(__amdgpu_buffer_rsrc_t rs, int voff, int soff) { return __builtin_amdgcn_raw_buffer_load_b32(rs, voff, soff, AUX); }
__device__ __forceinline__ void st16(__amdgpu_buffer_rsrc_t rs, int voff, v4u v) { __builtin_amdgcn_raw_buffer_store_b128(v, rs, voff, 0, AUX_SC1); }
#define RSRC(p, n) __builtin_amdgcn_make_buffer_rsrc((void*)(p), 0, (n), 0x00020000)
#define SCHED() __builtin_amdgcn_sched_barrier(0)
#define BARRIER() asm volatile("s_waitcnt lgkmcnt(0)\n\ts_barrier" ::: "memory")
#define MEMFENCE() asm volatile("" ::: "memory")
#define PIN(x) asm volatile("" : "+v"(x))

__device__ __forceinline__ float swap_add32(float a, float b) {
  auto s = __builtin_amdgcn_permlane32_swap(__float_as_uint(a), __float_as_uint(b), false, false);
  return __uint_as_float(s[0]) + __uint_as_float(s[1]);
}
__device__ __forceinline__ float swap_add16(float a, float b) {
  auto s = __builtin_amdgcn_permlane16_swap(__float_as_uint(a), __float_as_uint(b), false, false);
  return __uint_as_float(s[0]) + __uint_as_float(s[1]);
}
template <int CTRL> __device__ __forceinline__ float dpp(float x) { return __builtin_bit_cast(float, __builtin_amdgcn_mov_dpp(__builtin_bit_cast(int, x), CTRL, 0xf, 0xf, true)); }
constexpr int ROR8 = 0x128, ROR4 = 0x124, ROR2 = 0x122, ROR1 = 0x121;
constexpr int XOR1 = 0xB1, XOR2 = 0x4E, HMIR = 0x141;
__device__ __forceinline__ float xrow_max(float x) {
  auto s = __builtin_amdgcn_permlane16_swap(__float_as_uint(x), __float_as_uint(x), false, false);
  x = fmaxf(__uint_as_float(s[0]), __uint_as_float(s[1]));
  auto t = __builtin_amdgcn_permlane32_swap(__float_as_uint(x), __float_as_uint(x), false, false);
  return fmaxf(__uint_as_float(t[0]), __uint_as_float(t[1]));
}
__device__ __forceinline__ float xrow_sum(float x) {
  auto s = __builtin_amdgcn_permlane16_swap(__float_as_uint(x), __float_as_uint(x), false, false);
  x = __uint_as_float(s[0]) + __uint_as_float(s[1]);
  auto t = __builtin_amdgcn_permlane32_swap(__float_as_uint(x), __float_as_uint(x), false, false);
  return __uint_as_float(t[0]) + __uint_as_float(t[1]);
}
__device__ __forceinline__ float sum8(float x) { x += dpp<XOR1>(x); x += dpp<XOR2>(x); return x + dpp<HMIR>(x); }
__device__ __forceinline__ float sum_hi(float x) { x += dpp<ROR8>(x); return xrow_sum(x); }
__device__ __forceinline__ float max_hi(float x) { x = fmaxf(x, dpp<ROR8>(x)); return xrow_max(x); }
__device__ __forceinline__ float wave_sum(float x) { x += dpp<ROR4>(x); x += dpp<ROR2>(x); x += dpp<ROR1>(x); return sum_hi(x); }
__device__ __forceinline__ float wave_max(float x) { x = fmaxf(x, dpp<ROR4>(x)); x = fmaxf(x, dpp<ROR2>(x)); x = fmaxf(x, dpp<ROR1>(x)); return max_hi(x); }
__device__ __forceinline__ void sum4x8(float (&o)[8]) {
#define DPP8(M) "v_add_f32_dpp %0, %0, %0 " M " row_mask:0xf bank_mask:0xf\n\tv_add_f32_dpp %1, %1, %1 " M " row_mask:0xf bank_mask:0xf\n\tv_add_f32_dpp %2, %2, %2 " M " row_mask:0xf bank_mask:0xf\n\t" \
                "v_add_f32_dpp %3, %3, %3 " M " row_mask:0xf bank_mask:0xf\n\tv_add_f32_dpp %4, %4, %4 " M " row_mask:0xf bank_mask:0xf\n\tv_add_f32_dpp %5, %5, %5 " M " row_mask:0xf bank_mask:0xf\n\t" \
                "v_add_f32_dpp %6, %6, %6 " M " row_mask:0xf bank_mask:0xf\n\tv_add_f32_dpp %7, %7, %7 " M " row_mask:0xf bank_mask:0xf\n\t"
  asm volatile("s_nop 1\n\t" DPP8("quad_perm:[1,0,3,2]") DPP8("quad_perm:[2,3,0,1]") "s_nop 1" : "+v"(o[0]), "+v"(o[1]), "+v"(o[2]), "+v"(o[3]), "+v"(o[4]), "+v"(o[5]), "+v"(o[6]), "+v"(o[7]));
#undef DPP8
}
__device__ __forceinline__ float reduce4(f32x4 v) {
  const float x = swap_add16(swap_add32(v.x, v.z), swap_add32(v.y, v.w));
  return x + dpp<ROR8>(x);
}
__device__ __forceinline__ float reduce8(const float (&a)[8], int lane) {
  float t[4], u[2];
#pragma unroll
  for (int j = 0; j < 4; ++j) t[j] = swap_add32(a[j], a[j + 4]);
#pragma unroll
  for (int j = 0; j < 2; ++j) u[j] = swap_add16(t[j], t[j + 2]);
  const float v0 = u[0] + dpp<ROR8>(u[0]), v1 = u[1] + dpp<ROR8>(u[1]);
  return (lane & 8) ? v1 : v0;
}
__device__ __forceinline__ void fma8(float (&acc)[8], float xv, v4u w) {
#pragma unroll
  for (int j = 0; j < 4; ++j) { acc[2 * j] = fmaf(xv, bflo(w[j]), acc[2 * j]); acc[2 * j + 1] = fmaf(xv, bfhi(w[j]), acc[2 * j + 1]); }
}
__device__ __forceinline__ float dot2(unsigned a, unsigned b, float c) { return __builtin_amdgcn_fdot2_f32_bf16(__builtin_bit_cast(bf16x2, a), __builtin_bit_cast(bf16x2, b), c, false); }
__device__ __forceinline__ f32x4 mfma(v4u a, v4u b, f32x4 c) { return __builtin_amdgcn_mfma_f32_16x16x32_bf16(__builtin_bit_cast(bf16x8, a), __builtin_bit_cast(bf16x8, b), c, 0, 0, 0); }

struct Tags { unsigned t0, t1, t2, t3; };
__device__ __forceinline__ unsigned enc1(float v, unsigned t) { return ((__float_as_uint(v) + 512u) & ~1023u) | t; }
__device__ __forceinline__ v4u enc4(f32x4 v, const Tags& t) { return v4u{enc1(v.x, t.t0), enc1(v.y, t.t1), enc1(v.z, t.t2), enc1(v.w, t.t3)}; }
__device__ __forceinline__ unsigned orx(unsigned acc, unsigned x, unsigned t) { return __builtin_amdgcn_bitop3_b32(acc, x, t, 0xF6); }
__device__ __forceinline__ unsigned bad4(unsigned acc, v4u v, const Tags& t) { return orx(orx(orx(orx(acc, v.x, t.t0), v.y, t.t1), v.z, t.t2), v.w, t.t3); }
__device__ __forceinline__ f32x4 dec4(v4u v) { return __builtin_bit_cast(f32x4, v & ~1023u); }
__device__ __forceinline__ bool wave_ok(unsigned bad) { return __builtin_amdgcn_ballot_w64(bad != 0u) == 0ull; }

__device__ __forceinline__ void ctr_add(LAS unsigned char* L, int lane, unsigned v) {
  if (lane == 0) asm volatile("ds_add_u32 %0, %1" :: "v"((unsigned)(size_t)(L + L_CT)), "v"(v) : "memory");
}
__device__ __forceinline__ unsigned ctr_load(LAS unsigned char* L) { return __builtin_amdgcn_readfirstlane(__hip_atomic_load((LAS unsigned*)(L + L_CT), __ATOMIC_RELAXED, __HIP_MEMORY_SCOPE_WORKGROUP)); }
#define LDS4(off) (*(LAS f32x4*)(L + (off)))
#define LDSU4(off) (*(LAS v4u*)(L + (off)))
#define LDS1(off) (*(LAS float*)(L + (off)))
#define LA(off) ((int)(unsigned)(size_t)(L + (off)))
#define AT4(a) (*(LAS f32x4*)(size_t)(unsigned)(a))
#define AT1(a) (*(LAS float*)(size_t)(unsigned)(a))

__global__ void __launch_bounds__(NTHR, 2) fused_kernel(Params p) {
  extern __shared__ __attribute__((aligned(16))) unsigned char smem[];
  LAS unsigned char* L = (LAS unsigned char*)smem;
  const int tid = threadIdx.x, lane = tid & 63, w = __builtin_amdgcn_readfirstlane(tid >> 6), b = blockIdx.x;
  unsigned pf0 = 0;
  if (tid < PF_LINES) pf0 = ((const GAS unsigned*)(__builtin_amdgcn_s_getpc() & ~255ull))[tid * 32];
  SCHED();
  STAMP(0);
  const __amdgpu_buffer_rsrc_t rX = RSRC(p.x, 2048), rGW = RSRC(p.gw, 2048), rK = RSRC(p.K, 64 << 20), rV = RSRC(p.V, 64 << 20), rQ = RSRC(p.wq, 8 << 20), rO = RSRC(p.wo, 8 << 20),
                                rG = RSRC(p.wg, 7 << 20), rU = RSRC(p.wu, 7 << 20), rD = RSRC(p.wd, 7 << 20), rW = RSRC(p.ws, WS_END), rOut = RSRC(p.out, 2048);
  const int q_cg = b & 63, q_ks = b >> 6;
  const bool mlp = b < 224;
  const int g_cg = b % 28, g_ks = b / 28;
  const int hd = (b >> 3) & 3, sp = (b >> 5) * 8 + (b & 7);
  const int rg = (b & 7) + 8 * (b >> 5), cg4 = (b >> 3) & 3;

  const int qv = ((q_ks * 256 + w * 32 + (lane >> 3) * 4) * 4096 + q_cg * 64 + (lane & 7) * 8) * 2;
  v4u wqv[4];
#pragma unroll
  for (int n = 0; n < 4; ++n) wqv[n] = ld16<AUX_NT>(rQ, qv, n * 8192);
  SCHED();
  const v2u xq = ld8<0>(rX, (q_ks * 256 + w * 32 + (lane >> 3) * 4) * 2, 0);
  int vz = 0;
  PIN(vz);
  const unsigned E = ld4<AUX_SC1>(rW, vz, WS_EPOCH);
  const v2u xg = ld8<0>(rX, ((g_ks & 7) * 128 + w * 16 + (lane >> 4) * 4) * 2, 0);
  const unsigned xm = ld4<0>(rX, (w * 128 + lane * 2) * 2, 0), gm = ld4<0>(rGW, (w * 128 + lane * 2) * 2, 0);
  const int gv = mlp ? ((g_ks * 128 + w * 16 + (lane >> 4) * 4) * FF + g_cg * 128 + (lane & 15) * 8) * 2 : OOB;
  v4u gtv[4], upv[4];
  SCHED();
  asm volatile("s_barrier" ::: "memory");
#pragma unroll
  for (int n = 0; n < 4; ++n) gtv[n] = ld16<AUX_NT>(rG, gv, n * FF * 2);
  SCHED();
  {
    float acc[8] = {0.f, 0.f, 0.f, 0.f, 0.f, 0.f, 0.f, 0.f};
    fma8(acc, bflo(xq.x), wqv[0]); fma8(acc, bfhi(xq.x), wqv[1]); fma8(acc, bflo(xq.y), wqv[2]); fma8(acc, bfhi(xq.y), wqv[3]);
    STAMP(1);
    const float r = reduce8(acc, lane);
    LDS1(L_RQ + (w * 64 + (lane & 7) * 8 + (lane >> 3)) * 4) = r;
  }
  SCHED();
#pragma unroll
  for (int n = 0; n < 4; ++n) upv[n] = ld16<AUX_NT>(rU, gv, n * FF * 2);
  SCHED();
  const unsigned T = E + 1u;
  Tags tg;
  tg.t0 = T & 1023u; tg.t1 = (T + (T >> 10)) & 1023u; tg.t2 = (T + (T >> 20)) & 1023u; tg.t3 = (T + (T >> 30) + 341u) & 1023u;
  {
    const float s = wave_sum(rbf(bflo(xm) * bflo(gm)) + rbf(bfhi(xm) * bfhi(gm)));
    if (lane == 0) LDS1(L_GM + w * 4) = s;
    if (tid == 0) *(LAS unsigned*)(L + L_CT) = 0u;
  }
  asm volatile("" :: "v"(pf0));
  BARRIER();
  if (w == 0) {
    f32x4 s = LDS4(L_RQ + (lane & 15) * 16);
#pragma unroll
    for (int ww = 1; ww < 8; ++ww) s += LDS4(L_RQ + ww * 256 + (lane & 15) * 16);
    st16(rW, lane < 16 ? WS_QP + (q_ks * 4096 + q_cg * 64 + lane * 4) * 4 : OOB, enc4(s, tg));
  }
  float gmix;
  {
    const f32x4 a = LDS4(L_GM), c = LDS4(L_GM + 16);
    const float sg = rbf(((a.x + a.y) + (a.z + a.w)) + ((c.x + c.y) + (c.z + c.w)));
    gmix = rbf(1.f / (1.f + __expf(-sg)));
  }
  const int kvb = hd * (16 << 20) + (sp * 8 + w) * 8192;
  constexpr int TSTEP = 512 * 8192;
  const int lv = lane * 16;
  v4u kt0[8], kt1[8], kt2[8];
  {
    float ag[8] = {0.f, 0.f, 0.f, 0.f, 0.f, 0.f, 0.f, 0.f}, au[8] = {0.f, 0.f, 0.f, 0.f, 0.f, 0.f, 0.f, 0.f};
    fma8(ag, bflo(xg.x), gtv[0]); fma8(ag, bfhi(xg.x), gtv[1]); fma8(ag, bflo(xg.y), gtv[2]); fma8(ag, bfhi(xg.y), gtv[3]);
    SCHED();
#pragma unroll
    for (int j = 0; j < 4; ++j) kt0[j] = ld16<AUX_NT>(rK, lv, kvb + j * 1024);
    SCHED();
    fma8(au, bflo(xg.x), upv[0]); fma8(au, bfhi(xg.x), upv[1]); fma8(au, bflo(xg.y), upv[2]); fma8(au, bfhi(xg.y), upv[3]);
    SCHED();
#pragma unroll
    for (int j = 4; j < 8; ++j) kt0[j] = ld16<AUX_NT>(rK, lv, kvb + j * 1024);
    SCHED();
    float t[4], u[2];
#pragma unroll
    for (int j = 0; j < 4; ++j) t[j] = swap_add32(ag[j], ag[j + 4]);
#pragma unroll
    for (int j = 0; j < 2; ++j) u[j] = swap_add16(t[j], t[j + 2]);
    *(LAS f32x2*)(L + L_RG + ((w * 2 + 0) * 128 + (lane & 15) * 8 + (lane >> 4) * 2) * 4) = f32x2{u[0], u[1]};
#pragma unroll
    for (int j = 0; j < 4; ++j) t[j] = swap_add32(au[j], au[j + 4]);
#pragma unroll
    for (int j = 0; j < 2; ++j) u[j] = swap_add16(t[j], t[j + 2]);
    *(LAS f32x2*)(L + L_RG + ((w * 2 + 1) * 128 + (lane & 15) * 8 + (lane >> 4) * 2) * 4) = f32x2{u[0], u[1]};
  }
  BARRIER();
  if (w == 1) {
    const int mat = lane >> 5, pc = lane & 31;
    f32x4 s = LDS4(L_RG + (mat * 128 + pc * 4) * 4);
#pragma unroll
    for (int ww = 1; ww < 8; ++ww) s += LDS4(L_RG + ((ww * 2 + mat) * 128 + pc * 4) * 4);
    st16(rW, mlp ? WS_GU + ((mat * 8 + g_ks) * FF + g_cg * 128 + pc * 4) * 4 : OOB, enc4(s, tg));
  }
  {
    const int qo = WS_QP + ((lane >> 5) * 2 * 4096 + (hd * 8 + w) * 128 + (lane & 31) * 4) * 4;
    v4u q0 = ld16<AUX_SC1>(rW, qo, 0), q1 = ld16<AUX_SC1>(rW, qo, 4096 * 4);
    SCHED();
#pragma unroll
    for (int j = 0; j < 8; ++j) kt1[j] = ld16<AUX_NT>(rK, lv, kvb + TSTEP + j * 1024);
#pragma unroll
    for (int j = 0; j < 8; ++j) kt2[j] = ld16<AUX_NT>(rK, lv, kvb + 2 * TSTEP + j * 1024);
    SCHED();
    if (!wave_ok(bad4(bad4(0u, q0, tg), q1, tg) & 1023u)) {
#pragma unroll 1
      for (unsigned spins = 0; spins < SPIN_CAP; ++spins) {
        __builtin_amdgcn_s_sleep(1);
        MEMFENCE();
        q0 = ld16<AUX_SC1>(rW, qo, 0); q1 = ld16<AUX_SC1>(rW, qo, 4096 * 4);
        if (wave_ok(bad4(bad4(0u, q0, tg), q1, tg) & 1023u)) break;
      }
    }
    const f32x4 a = dec4(q0) + dec4(q1);
    f32x4 s;
    s.x = swap_add32(a.x, a.x); s.y = swap_add32(a.y, a.y); s.z = swap_add32(a.z, a.z); s.w = swap_add32(a.w, a.w);
    if (lane < 32) *(LAS v2u*)(L + L_QS + (w * 128 + lane * 4) * 2) = v2u{pkbf(s.x, s.y), pkbf(s.z, s.w)};
  }
  BARRIER();
  STAMP(4);

  const int g4 = lane >> 4, n16 = lane & 15;
  v4u qf[4];
#pragma unroll
  for (int s = 0; s < 4; ++s) qf[s] = LDSU4(L_QS + ((n16 & 7) * 128 + (4 * s + g4) * 8) * 2);
  f32x4 S[8];
  const int kb = L_KB + w * 8192;
  auto qk_tile = [&](const v4u (&kt)[8], int t) {
#pragma unroll
    for (int j = 0; j < 8; ++j) { const int r = 4 * g4 + (j & 3); LDSU4(kb + ((j >> 2) * 16 + r) * 256 + ((n16 ^ r) << 4)) = kt[j]; }
#pragma unroll
    for (int T2 = 0; T2 < 2; ++T2) {
      f32x4 c = {0.f, 0.f, 0.f, 0.f};
#pragma unroll
      for (int s = 0; s < 4; ++s) c = mfma(LDSU4(kb + (T2 * 16 + n16) * 256 + (((4 * s + g4) ^ n16) << 4)), qf[s], c);
      S[2 * t + T2] = c;
    }
  };
  v4u kt3[8], vt0[8], vt1[8], vt2[8], vt3[8];
  qk_tile(kt0, 0);
  SCHED();
#pragma unroll
  for (int j = 0; j < 8; ++j) kt3[j] = ld16<AUX_NT>(rK, lv, kvb + 3 * TSTEP + j * 1024);
  SCHED();
  qk_tile(kt1, 1);
  SCHED();
#pragma unroll
  for (int j = 0; j < 8; ++j) vt0[j] = ld16<AUX_NT>(rV, lv, kvb + j * 1024);
  SCHED();
  qk_tile(kt2, 2);
  SCHED();
#pragma unroll
  for (int j = 0; j < 8; ++j) vt1[j] = ld16<AUX_NT>(rV, lv, kvb + TSTEP + j * 1024);
  SCHED();
  qk_tile(kt3, 3);
  SCHED();
  STAMP(5);
  float mw;
  {
    float mx = fmaxf(fmaxf(S[0].x, S[0].y), fmaxf(S[0].z, S[0].w));
#pragma unroll
    for (int i = 1; i < 8; ++i) mx = fmaxf(mx, fmaxf(fmaxf(S[i].x, S[i].y), fmaxf(S[i].z, S[i].w)));
    mw = xrow_max(mx) * SCL;
    if (lane < 8) LDS1(L_MS + (w * 8 + lane) * 4) = mw;
    ctr_add(L, lane, 1u);
  }
  float lsum = 0.f;
  v4u pf[4];
  auto soft_tile = [&](int t, float ref) {
    float e[8];
#pragma unroll
    for (int i = 0; i < 4; ++i) { e[i] = __builtin_amdgcn_exp2f(fmaf(S[2 * t][i], SCL, -ref)); e[4 + i] = __builtin_amdgcn_exp2f(fmaf(S[2 * t + 1][i], SCL, -ref)); }
    lsum += ((e[0] + e[1]) + (e[2] + e[3])) + ((e[4] + e[5]) + (e[6] + e[7]));
    pf[t] = v4u{pkbf(e[0], e[1]), pkbf(e[2], e[3]), pkbf(e[4], e[5]), pkbf(e[6], e[7])};
  };
  soft_tile(0, mw); soft_tile(1, mw);
  f32x4 O[8];
#pragma unroll
  for (int i = 0; i < 8; ++i) O[i] = f32x4{0.f, 0.f, 0.f, 0.f};
  auto pv_tile = [&](const v4u (&vt)[8], int t) {
#pragma unroll
    for (int i = 0; i < 8; ++i) {
      const unsigned sel = (i & 1) ? 0x07060302u : 0x05040100u;
      const v4u bf = {__builtin_amdgcn_perm(vt[1][i >> 1], vt[0][i >> 1], sel), __builtin_amdgcn_perm(vt[3][i >> 1], vt[2][i >> 1], sel),
                      __builtin_amdgcn_perm(vt[5][i >> 1], vt[4][i >> 1], sel), __builtin_amdgcn_perm(vt[7][i >> 1], vt[6][i >> 1], sel)};
      O[i] = mfma(pf[t], bf, O[i]);
    }
  };
  pv_tile(vt0, 0);
  SCHED();
#pragma unroll
  for (int j = 0; j < 8; ++j) vt2[j] = ld16<AUX_NT>(rV, lv, kvb + 2 * TSTEP + j * 1024);
  SCHED();
  pv_tile(vt1, 1);
  SCHED();
#pragma unroll
  for (int j = 0; j < 8; ++j) vt3[j] = ld16<AUX_NT>(rV, lv, kvb + 3 * TSTEP + j * 1024);
  SCHED();
  if (w == 0) {
    const int h_mat = lane / 28, h_kh = (lane % 28) / 14, h_pc = lane % 14;
    const int ho = lane < 56 ? WS_GU + ((h_mat * 8 + h_kh * 4) * FF + rg * 56 + h_pc * 4) * 4 : OOB;
    const unsigned hmask = lane < 56 ? 1023u : 0u;
    v4u hv[4];
#pragma unroll 1
    for (unsigned spins = 0; spins < SPIN_CAP; ++spins) {
      MEMFENCE();
#pragma unroll
      for (int i = 0; i < 4; ++i) hv[i] = ld16<AUX_SC1>(rW, ho, i * FF * 4);
      if (wave_ok(bad4(bad4(bad4(bad4(0u, hv[0], tg), hv[1], tg), hv[2], tg), hv[3], tg) & hmask)) break;
      __builtin_amdgcn_s_sleep(8);
    }
    const f32x4 s = (dec4(hv[0]) + dec4(hv[1])) + (dec4(hv[2]) + dec4(hv[3]));
    if (lane < 56) LDS4(L_GS + lane * 16) = s;
    MEMFENCE();
    float hval = 0.f;
    if (lane < 56) {
      const float gsum = rbf(LDS1(L_GS + lane * 4) + LDS1(L_GS + 14 * 16 + lane * 4));
      const float usum = rbf(LDS1(L_GS + 28 * 16 + lane * 4) + LDS1(L_GS + 42 * 16 + lane * 4));
      hval = rbf((gsum > 0.f ? gsum : 0.f) * usum) * rbf(1.f - gmix);
    }
    LDS1(L_HS + lane * 4) = hval;
  }
  SCHED();
  ctr_add(L, lane, 1u << 8);
  SCHED();
#pragma unroll 1
  for (unsigned spins = 0; spins < SPIN_CAP; ++spins) {
    if ((ctr_load(L) & 255u) >= 8u) break;
    __builtin_amdgcn_s_sleep(4);
  }
  MEMFENCE();
  {
    const float mcu = max_hi(LDS1(L_MS + (((lane >> 3) & 7) * 8 + (lane & 7)) * 4));
    const f32x4 mo = LDS4(L_MS + (w * 8 + (g4 & 1) * 4) * 4);
    f32x4 mn = LDS4(L_MS + ((g4 & 1) * 4) * 4);
#pragma unroll
    for (int ww = 1; ww < 8; ++ww) { const f32x4 t = LDS4(L_MS + (ww * 8 + (g4 & 1) * 4) * 4); mn = f32x4{fmaxf(mn.x, t.x), fmaxf(mn.y, t.y), fmaxf(mn.z, t.z), fmaxf(mn.w, t.w)}; }
    lsum *= __builtin_amdgcn_exp2f(mw - mcu);
    soft_tile(2, mcu); soft_tile(3, mcu);
    const f32x4 fo = {__builtin_amdgcn_exp2f(mo.x - mn.x), __builtin_amdgcn_exp2f(mo.y - mn.y), __builtin_amdgcn_exp2f(mo.z - mn.z), __builtin_amdgcn_exp2f(mo.w - mn.w)};
#pragma unroll
    for (int i = 0; i < 8; ++i) { O[i] *= fo; PIN(O[i]); }
    lsum = xrow_sum(lsum);
    if (lane < 8) { LDS1(L_LS + (w * 8 + lane) * 4) = lsum; LDS1(L_MC + lane * 4) = mcu; }
    PIN(pf[2]); PIN(pf[3]);
  }
  const int j4 = w & 3, hb = lane >> 5;
  const int hq = rg >> 1, half = rg & 1, s_ = (lane >> 3) * 8 + 2 * j4;
  int a_ow = LA(kb + ((g4 & 1) * 4 * 128 + n16 * 8 + (g4 >> 1) * 4) * 4);
  int a_or = LA(L_KB + ((2 * j4 + hb) * 128 + (lane & 31) * 4) * 4);
  int a_lr = LA(L_LS + ((lane & 7) * 8 + 2 * j4 + hb) * 4);
  int a_mc = LA(L_MC + (2 * j4 + hb) * 4);
  int a_op = WS_OP + ((((hd * 8 + 2 * j4 + hb) * 2 + ((lane & 31) >> 4)) * 64 + sp) * 64 + (lane & 15) * 4) * 4;
  int a_ml = (lane & 31) == 0 ? WS_ML + ((hd * 8 + 2 * j4 + hb) * 64 + sp) * 16 : OOB;
  int a_ma = WS_ML + (hq * 64 + lane) * 16;
  int a_mo = WS_ML + (hq * 64 + s_) * 16;
  int a_oo = WS_OP + (((hq * 2 + half) * 64 + s_) * 64 + (lane & 7) * 4) * 4;
  int a_rcw = LA(L_RC + (j4 * 64 + 4 * ((lane & 7) + 8 * (lane >> 5)) + ((lane >> 3) & 3)) * 4);
  int a_rcr = LA(L_RC + ((lane & 3) * 64 + j4 * 16 + hb * 8) * 4);
  int a_rfw = LA(L_RF + (j4 * 256 + (lane & 31) * 8 + hb * 4) * 4);
  int a_rfr = LA(L_RF + (g4 * 256 + j4 * 64 + n16 * 4) * 4);
  int a_fp = WS_FP + ((cg4 * 64 + rg) * 256 + j4 * 64 + n16 * 4 + g4) * 4;
  int a_hs = LA(L_HS + (j4 * 14 + hb * 7) * 4);
  unsigned tl_t = g4 == 0 ? tg.t0 : g4 == 1 ? tg.t1 : g4 == 2 ? tg.t2 : tg.t3;
  PIN(tl_t);
  PIN(a_ow); PIN(a_or); PIN(a_lr); PIN(a_mc); PIN(a_op); PIN(a_ml); PIN(a_ma); PIN(a_mo); PIN(a_oo); PIN(a_rcw); PIN(a_rcr); PIN(a_rfw); PIN(a_rfr); PIN(a_fp); PIN(a_hs);
  SCHED();
  pv_tile(vt2, 2);
  SCHED();
  pv_tile(vt3, 3);
  STAMP(6);
#pragma unroll
  for (int r = 0; r < 4; ++r) {
    const f32x4 v = g4 < 2 ? f32x4{O[0][r], O[1][r], O[2][r], O[3][r]} : f32x4{O[4][r], O[5][r], O[6][r], O[7][r]};
    AT4(a_ow + r * 512) = v;
  }
  ctr_add(L, lane, 1u << 16);
  if (w >= 4) {
#pragma unroll 1
    for (unsigned spins = 0; spins < SPIN_CAP; ++spins) { if (ctr_load(L) >= (8u << 16)) break; }
    MEMFENCE();
    {
      f32x4 part[8];
#pragma unroll
      for (int ww = 0; ww < 8; ++ww) part[ww] = AT4(a_or + ww * 8192);
      const float lw = AT1(a_lr), mh = AT1(a_mc);
      SCHED();
      const float Ls = sum8(lw);
      const f32x4 acc = ((part[0] + part[1]) + (part[2] + part[3])) + ((part[4] + part[5]) + (part[6] + part[7]));
      st16(rW, a_op, enc4(acc, tg));
      st16(rW, a_ml, v4u{__float_as_uint(mh), T, __float_as_uint(Ls), T});
      STAMP(7);
    }
    SCHED();
    asm volatile("s_waitcnt vmcnt(0)" ::: "memory");
    v4u ma = ld16<AUX_SC1>(rW, a_ma, 0), m0 = ld16<AUX_SC1>(rW, a_mo, 0), m1 = ld16<AUX_SC1>(rW, a_mo, 16);
    v4u o0 = ld16<AUX_SC1>(rW, a_oo, 0), o1 = ld16<AUX_SC1>(rW, a_oo, 128), o2 = ld16<AUX_SC1>(rW, a_oo, 256), o3 = ld16<AUX_SC1>(rW, a_oo, 384);
    SCHED();
    ctr_add(L, lane, 1u << 24);
    SCHED();
    unsigned bad = ((((ma.y ^ T) | (ma.w ^ T)) | ((m0.y ^ T) | (m0.w ^ T))) | ((m1.y ^ T) | (m1.w ^ T))) | (bad4(bad4(bad4(bad4(0u, o0, tg), o1, tg), o2, tg), o3, tg) & 1023u);
    if (!wave_ok(bad)) {
#pragma unroll 1
      for (unsigned spins = 0; spins < SPIN_CAP; ++spins) {
        MEMFENCE();
        const bool again = bad != 0u;
        const int xa = again ? a_ma : OOB, xm2 = again ? a_mo : OOB, xo = again ? a_oo : OOB;
        const v4u na = ld16<AUX_SC1>(rW, xa, 0), n0 = ld16<AUX_SC1>(rW, xm2, 0), n1 = ld16<AUX_SC1>(rW, xm2, 16);
        const v4u p0 = ld16<AUX_SC1>(rW, xo, 0), p1 = ld16<AUX_SC1>(rW, xo, 128), p2 = ld16<AUX_SC1>(rW, xo, 256), p3 = ld16<AUX_SC1>(rW, xo, 384);
        ma = again ? na : ma; m0 = again ? n0 : m0; m1 = again ? n1 : m1; o0 = again ? p0 : o0; o1 = again ? p1 : o1; o2 = again ? p2 : o2; o3 = again ? p3 : o3;
        bad = ((((ma.y ^ T) | (ma.w ^ T)) | ((m0.y ^ T) | (m0.w ^ T))) | ((m1.y ^ T) | (m1.w ^ T))) | (bad4(bad4(bad4(bad4(0u, o0, tg), o1, tg), o2, tg), o3, tg) & 1023u);
        if (wave_ok(bad)) break;
      }
    }
    STAMP(3);
    const float M = wave_max(__uint_as_float(ma.x));
    const float inv = __builtin_amdgcn_rcpf(wave_sum(__builtin_amdgcn_exp2f(__uint_as_float(ma.x) - M) * __uint_as_float(ma.z)));
    const float w0 = __builtin_amdgcn_exp2f(__uint_as_float(m0.x) - M) * inv, w1 = __builtin_amdgcn_exp2f(__uint_as_float(m1.x) - M) * inv;
    const f32x4 a0 = dec4(o0) * w0 + dec4(o2) * w1, a1 = dec4(o1) * w0 + dec4(o3) * w1;
    const float vals[8] = {a0.x, a0.y, a0.z, a0.w, a1.x, a1.y, a1.z, a1.w};
    AT1(a_rcw) = reduce8(vals, lane);
    BARRIER();
    STAMP(8);
  } else {
    const int ov = ((rg * 64 + j4 * 16 + hb * 8) * 1024 + cg4 * 256 + (lane & 31) * 8) * 2;
    const int dv = ((rg * 56 + j4 * 14 + hb * 7) * 1024 + cg4 * 256 + (lane & 31) * 8) * 2;
#pragma unroll 1
    for (unsigned spins = 0; spins < SPIN_CAP; ++spins) {
      if (((ctr_load(L) >> 8) & 255u) >= 8u) break;
      __builtin_amdgcn_s_sleep(4);
    }
    MEMFENCE();
    v4u wdv[7], wov[8];
#pragma unroll
    for (int n = 0; n < 7; ++n) wdv[n] = ld16<AUX_NT>(rD, dv, n * 2048);
#pragma unroll
    for (int n = 0; n < 4; ++n) wov[n] = ld16<AUX_NT>(rO, ov, n * 2048);
    SCHED();
    float accd[4];
    {
      float acc[8] = {0.f, 0.f, 0.f, 0.f, 0.f, 0.f, 0.f, 0.f};
#pragma unroll
      for (int n = 0; n < 7; ++n) fma8(acc, AT1(a_hs + n * 4), wdv[n]);
#pragma unroll
      for (int j = 0; j < 4; ++j) { accd[j] = swap_add32(acc[j], acc[j + 4]); PIN(accd[j]); }
    }
    SCHED();
#pragma unroll 1
    for (unsigned spins = 0; spins < SPIN_CAP; ++spins) { if ((ctr_load(L) >> 24) >= 4u) break; __builtin_amdgcn_s_sleep(1); }
    MEMFENCE();
#pragma unroll
    for (int n = 4; n < 8; ++n) wov[n] = ld16<AUX_NT>(rO, ov, n * 2048);
    SCHED();
    unsigned wp[4][8];
#pragma unroll
    for (int pr = 0; pr < 2; ++pr)
#pragma unroll
      for (int j = 0; j < 4; ++j) { wp[pr][2 * j] = __builtin_amdgcn_perm(wov[2 * pr + 1][j], wov[2 * pr][j], 0x05040100u); wp[pr][2 * j + 1] = __builtin_amdgcn_perm(wov[2 * pr + 1][j], wov[2 * pr][j], 0x07060302u); PIN(wp[pr][2 * j]); PIN(wp[pr][2 * j + 1]); }
    float ao[8] = {0.f, 0.f, 0.f, 0.f, 0.f, 0.f, 0.f, 0.f};
#pragma unroll
    for (int j = 0; j < 8; ++j) PIN(ao[j]);
    STAMP(2);
    BARRIER();
    {
      const f32x4 pa = AT4(a_rcr), pb = AT4(a_rcr + 16);
      float o[8] = {pa.x, pa.y, pa.z, pa.w, pb.x, pb.y, pb.z, pb.w};
      sum4x8(o);
#pragma unroll
      for (int pr = 0; pr < 4; ++pr) {
        const unsigned op = pkbf(o[2 * pr], o[2 * pr + 1]);
        if (pr >= 2) {
#pragma unroll
          for (int j = 0; j < 4; ++j) { wp[pr][2 * j] = __builtin_amdgcn_perm(wov[2 * pr + 1][j], wov[2 * pr][j], 0x05040100u); wp[pr][2 * j + 1] = __builtin_amdgcn_perm(wov[2 * pr + 1][j], wov[2 * pr][j], 0x07060302u); }
        }
#pragma unroll
        for (int j = 0; j < 8; ++j) ao[j] = dot2(wp[pr][j], op, ao[j]);
      }
      f32x4 v;
      v.x = fmaf(gmix, swap_add32(ao[0], ao[4]), accd[0]); v.y = fmaf(gmix, swap_add32(ao[1], ao[5]), accd[1]);
      v.z = fmaf(gmix, swap_add32(ao[2], ao[6]), accd[2]); v.w = fmaf(gmix, swap_add32(ao[3], ao[7]), accd[3]);
      AT4(a_rfw) = v;
    }
  }
  if (w >= 4) {
    return;
  }
  BARRIER();
  {
    const f32x4 pc = AT4(a_rfr);
    const float s = swap_add16(swap_add32(pc.x, pc.z), swap_add32(pc.y, pc.w));
    __builtin_amdgcn_raw_buffer_store_b32(enc1(s, tl_t), rW, a_fp, 0, AUX_SC1);
    STAMP(9);
  }
  if (b < 32 && w <= 1) {
    if (w == 1) __builtin_amdgcn_s_sleep(5);
    const int c = b >> 3, seg = b & 7;
    const int fo = WS_FP + ((c * 64 + (lane >> 3)) * 256 + seg * 32 + (lane & 7) * 4) * 4;
    const int oo = (lane & 8) ? OOB : (c * 256 + seg * 32 + (lane & 7) * 4 + (lane >> 4)) * 2;
    v4u f[8];
    asm volatile("s_waitcnt vmcnt(0)" ::: "memory");
#pragma unroll 1
    for (unsigned spins = 0; spins < SPIN_CAP; ++spins) {
      MEMFENCE();
#pragma unroll
      for (int n = 0; n < 8; ++n) f[n] = ld16<AUX_SC1>(rW, fo, n * 8192);
      unsigned bad = 0;
#pragma unroll
      for (int n = 0; n < 8; ++n) bad = bad4(bad, f[n], tg);
      if (wave_ok(bad & 1023u)) break;
    }
    STAMP(10);
    if (b == 0 && w == 0) __builtin_amdgcn_raw_buffer_store_b32(T, rW, lane == 0 ? WS_EPOCH : OOB, 0, AUX_SC1);
    const f32x4 d = ((dec4(f[0]) + dec4(f[1])) + (dec4(f[2]) + dec4(f[3]))) + ((dec4(f[4]) + dec4(f[5])) + (dec4(f[6]) + dec4(f[7])));
    const float r = reduce4(d);
    __builtin_amdgcn_raw_buffer_store_b16((unsigned short)pkbf(r, r), rOut, oo, 0, 0);
  }
}

}

#define NSTAMP_MAX 1

extern "C" void kernel_launch(void* const* d_in, const int* in_sizes, int n_in, void* d_out, int out_size, void* d_ws, size_t ws_size, hipStream_t stream) {
  fk::Params p;
  p.x = d_in[0]; p.K = d_in[1]; p.V = d_in[2]; p.wq = d_in[3]; p.wo = d_in[4];
  p.wg = d_in[6]; p.wu = d_in[7]; p.wd = d_in[8]; p.gw = d_in[9];
  p.out = d_out; p.ws = (unsigned char*)d_ws;
  (void)hipFuncSetAttribute((const void*)fk::fused_kernel, hipFuncAttributeMaxDynamicSharedMemorySize, fk::LDS_BYTES);
  hipLaunchKernelGGL(fk::fused_kernel, dim3(fk::NWG), dim3(fk::NTHR), fk::LDS_BYTES, stream, p);
}
```

```cpp
#include <hip/hip_runtime.h>
#include <stdint.h>

namespace fk {

typedef unsigned v4u __attribute__((ext_vector_type(4)));
typedef unsigned v2u __attribute__((ext_vector_type(2)));
typedef float f32x4 __attribute__((ext_vector_type(4)));
typedef float f32x2 __attribute__((ext_vector_type(2)));
typedef __bf16 bf16x8 __attribute__((ext_vector_type(8)));
typedef __bf16 bf16x2 __attribute__((ext_vector_type(2)));
#define LAS __attribute__((address_space(3)))
#define GAS __attribute__((address_space(1)))

constexpr int NWG = 256, NTHR = 512;
constexpr int FF = 3584;
constexpr int WS_EPOCH = 0;
constexpr int WS_QP = 4096;
constexpr int WS_GU = WS_QP + 4 * 4096 * 4;
constexpr int WS_ML = WS_GU + 2 * 8 * FF * 4;
constexpr int WS_OP = WS_ML + 32 * 64 * 16;
constexpr int WS_FP = WS_OP + 32 * 2 * 64 * 64 * 4;
constexpr int WS_END = WS_FP + 4 * 64 * 256 * 4;
constexpr int L_KB = 0;
constexpr int L_RQ = 65536;
constexpr int L_RG = L_RQ + 2048;
constexpr int L_RC = L_RG + 8192;
constexpr int L_RF = L_RC + 2048;
constexpr int L_QS = L_RF + 8192;
constexpr int L_MS = L_QS + 2048;
constexpr int L_LS = L_MS + 256;
constexpr int L_MC = L_LS + 256;
constexpr int L_CT = L_MC + 32;
constexpr int L_GS = L_CT + 32;
constexpr int L_HS = L_GS + 1024;
constexpr int L_GM = L_HS + 256;
constexpr int L_END = L_GM + 32;
constexpr int LDS_BYTES = 90112;
static_assert(L_END <= LDS_BYTES, "lds");

constexpr int AUX_NT = 2, AUX_SC1 = 16;
constexpr int OOB = 0x7ffffff0;
constexpr unsigned SPIN_CAP = 1u << 18;
constexpr float SCL = 0.08838834764831845f * 1.4426950408889634f;
constexpr int PF_LINES = 125;

struct Params {
  const void *x, *K, *V, *wq, *wo, *wg, *wu, *wd, *gw;
  void* out;
  unsigned char* ws;
};

__device__ __forceinline__ float bflo(unsigned u) { return __uint_as_float(u << 16); }
__device__ __forceinline__ float bfhi(unsigned u) { return __uint_as_float(u & 0xffff0000u); }
__device__ __forceinline__ unsigned pkbf(float a, float b) { f32x2 v = {a, b}; return __builtin_bit_cast(unsigned, __builtin_convertvector(v, bf16x2)); }
__device__ __forceinline__ float rbf(float a) { return bflo(pkbf(a, 0.f)); }
template <int AUX> __device__ __forceinline__ v4u ld16(__amdgpu_buffer_rsrc_t rs, int voff, int soff) { return __builtin_bit_cast(v4u, __builtin_amdgcn_raw_buffer_load_b128(rs, voff, soff, AUX)); }
template <int AUX> __device__ __forceinline__ v2u ld8(__amdgpu_buffer_rsrc_t rs, int voff, int soff) { return __builtin_bit_cast(v2u, __builtin_amdgcn_raw_buffer_load_b64(rs, voff, soff, AUX)); }
template <int AUX> __device__ __forceinline__ unsigned ld4(__amdgpu_buffer_rsrc_t rs, int voff, int soff) { return __builtin_amdgcn_raw_buffer_load_b32(rs, voff, soff, AUX); }
__device__ __forceinline__ void st16(__amdgpu_buffer_rsrc_t rs, int voff, v4u v) { __builtin_amdgcn_raw_buffer_store_b128(v, rs, voff, 0, AUX_SC1); }
#define RSRC(p, n) __builtin_amdgcn_make_buffer_rsrc((void*)(p), 0, (n), 0x00020000)
#define SCHED() __builtin_amdgcn_sched_barrier(0)
#define BARRIER() asm volatile("s_waitcnt lgkmcnt(0)\n\ts_barrier" ::: "memory")
#define MEMFENCE() asm volatile("" ::: "memory")
#define PIN(x) asm volatile("" : "+v"(x))

__device__ __forceinline__ float swap_add32(float a, float b) {
  auto s = __builtin_amdgcn_permlane32_swap(__float_as_uint(a), __float_as_uint(b), false, false);
  return __uint_as_float(s[0]) + __uint_as_float(s[1]);
}
__device__ __forceinline__ float swap_add16(float a, float b) {
  auto s = __builtin_amdgcn_permlane16_swap(__float_as_uint(a), __float_as_uint(b), false, false);
  return __uint_as_float(s[0]) + __uint_as_float(s[1]);
}
template <int CTRL> __device__ __forceinline__ float dpp(float x) { return __builtin_bit_cast(float, __builtin_amdgcn_mov_dpp(__builtin_bit_cast(int, x), CTRL, 0xf, 0xf, true)); }
constexpr int ROR8 = 0x128, ROR4 = 0x124, ROR2 = 0x122, ROR1 = 0x121;
constexpr int XOR1 = 0xB1, XOR2 = 0x4E, HMIR = 0x141;
__device__ __forceinline__ float xrow_max(float x) {
  auto s = __builtin_amdgcn_permlane16_swap(__float_as_uint(x), __float_as_uint(x), false, false);
  x = fmaxf(__uint_as_float(s[0]), __uint_as_float(s[1]));
  auto t = __builtin_amdgcn_permlane32_swap(__float_as_uint(x), __float_as_uint(x), false, false);
  return fmaxf(__uint_as_float(t[0]), __uint_as_float(t[1]));
}
__device__ __forceinline__ float xrow_sum(float x) {
  auto s = __builtin_amdgcn_permlane16_swap(__float_as_uint(x), __float_as_uint(x), false, false);
  x = __uint_as_float(s[0]) + __uint_as_float(s[1]);
  auto t = __builtin_amdgcn_permlane32_swap(__float_as_uint(x), __float_as_uint(x), false, false);
  return __uint_as_float(t[0]) + __uint_as_float(t[1]);
}
__device__ __forceinline__ float sum8(float x) { x += dpp<XOR1>(x); x += dpp<XOR2>(x); return x + dpp<HMIR>(x); }
__device__ __forceinline__ float sum_hi(float x) { x += dpp<ROR8>(x); return xrow_sum(x); }
__device__ __forceinline__ float max_hi(float x) { x = fmaxf(x, dpp<ROR8>(x)); return xrow_max(x); }
__device__ __forceinline__ float wave_sum(float x) { x += dpp<ROR4>(x); x += dpp<ROR2>(x); x += dpp<ROR1>(x); return sum_hi(x); }
__device__ __forceinline__ float wave_max(float x) { x = fmaxf(x, dpp<ROR4>(x)); x = fmaxf(x, dpp<ROR2>(x)); x = fmaxf(x, dpp<ROR1>(x)); return max_hi(x); }
__device__ __forceinline__ void sum4x8(float (&o)[8]) {
#define DPP8(M) "v_add_f32_dpp %0, %0, %0 " M " row_mask:0xf bank_mask:0xf\n\tv_add_f32_dpp %1, %1, %1 " M " row_mask:0xf bank_mask:0xf\n\tv_add_f32_dpp %2, %2, %2 " M " row_mask:0xf bank_mask:0xf\n\t" \
                "v_add_f32_dpp %3, %3, %3 " M " row_mask:0xf bank_mask:0xf\n\tv_add_f32_dpp %4, %4, %4 " M " row_mask:0xf bank_mask:0xf\n\tv_add_f32_dpp %5, %5, %5 " M " row_mask:0xf bank_mask:0xf\n\t" \
                "v_add_f32_dpp %6, %6, %6 " M " row_mask:0xf bank_mask:0xf\n\tv_add_f32_dpp %7, %7, %7 " M " row_mask:0xf bank_mask:0xf\n\t"
  asm volatile("s_nop 1\n\t" DPP8("quad_perm:[1,0,3,2]") DPP8("quad_perm:[2,3,0,1]") "s_nop 1" : "+v"(o[0]), "+v"(o[1]), "+v"(o[2]), "+v"(o[3]), "+v"(o[4]), "+v"(o[5]), "+v"(o[6]), "+v"(o[7]));
#undef DPP8
}
__device__ __forceinline__ float reduce4(f32x4 v) {
  const float x = swap_add16(swap_add32(v.x, v.z), swap_add32(v.y, v.w));
  return x + dpp<ROR8>(x);
}
__device__ __forceinline__ float reduce8(const float (&a)[8], int lane) {
  float t[4], u[2];
#pragma unroll
  for (int j = 0; j < 4; ++j) t[j] = swap_add32(a[j], a[j + 4]);
#pragma unroll
  for (int j = 0; j < 2; ++j) u[j] = swap_add16(t[j], t[j + 2]);
  const float v0 = u[0] + dpp<ROR8>(u[0]), v1 = u[1] + dpp<ROR8>(u[1]);
  return (lane & 8) ? v1 : v0;
}
__device__ __forceinline__ void fma8(float (&acc)[8], float xv, v4u w) {
#pragma unroll
  for (int j = 0; j < 4; ++j) { acc[2 * j] = fmaf(xv, bflo(w[j]), acc[2 * j]); acc[2 * j + 1] = fmaf(xv, bfhi(w[j]), acc[2 * j + 1]); }
}
__device__ __forceinline__ float dot2(unsigned a, unsigned b, float c) { return __builtin_amdgcn_fdot2_f32_bf16(__builtin_bit_cast(bf16x2, a), __builtin_bit_cast(bf16x2, b), c, false); }
__device__ __forceinline__ f32x4 mfma(v4u a, v4u b, f32x4 c) { return __builtin_amdgcn_mfma_f32_16x16x32_bf16(__builtin_bit_cast(bf16x8, a), __builtin_bit_cast(bf16x8, b), c, 0, 0, 0); }

struct Tags { unsigned t0, t1, t2, t3; };
__device__ __forceinline__ unsigned enc1(float v, unsigned t) { return ((__float_as_uint(v) + 512u) & ~1023u) | t; }
__device__ __forceinline__ v4u enc4(f32x4 v, const Tags& t) { return v4u{enc1(v.x, t.t0), enc1(v.y, t.t1), enc1(v.z, t.t2), enc1(v.w, t.t3)}; }
__device__ __forceinline__ unsigned orx(unsigned acc, unsigned x, unsigned t) { return __builtin_amdgcn_bitop3_b32(acc, x, t, 0xF6); }
__device__ __forceinline__ unsigned bad4(unsigned acc, v4u v, const Tags& t) { return orx(orx(orx(orx(acc, v.x, t.t0), v.y, t.t1), v.z, t.t2), v.w, t.t3); }
__device__ __forceinline__ f32x4 dec4(v4u v) { return __builtin_bit_cast(f32x4, v & ~1023u); }
__device__ __forceinline__ bool wave_ok(unsigned bad) { return __builtin_amdgcn_ballot_w64(bad != 0u) == 0ull; }

__device__ __forceinline__ void ctr_add(LAS unsigned char* L, int lane, unsigned v) {
  if (lane == 0) asm volatile("ds_add_u32 %0, %1" :: "v"((unsigned)(size_t)(L + L_CT)), "v"(v) : "memory");
}
__device__ __forceinline__ unsigned ctr_load(LAS unsigned char* L) { return __builtin_amdgcn_readfirstlane(__hip_atomic_load((LAS unsigned*)(L + L_CT), __ATOMIC_RELAXED, __HIP_MEMORY_SCOPE_WORKGROUP)); }
#define LDS4(off) (*(LAS f32x4*)(L + (off)))
#define LDSU4(off) (*(LAS v4u*)(L + (off)))
#define LDS1(off) (*(LAS float*)(L + (off)))
#define LA(off) ((int)(unsigned)(size_t)(L + (off)))
#define AT4(a) (*(LAS f32x4*)(size_t)(unsigned)(a))
#define AT1(a) (*(LAS float*)(size_t)(unsigned)(a))

__global__ void __launch_bounds__(NTHR, 2) fused_kernel(Params p) {
  extern __shared__ __attribute__((aligned(16))) unsigned char smem[];
  LAS unsigned char* L = (LAS unsigned char*)smem;
  const int tid = threadIdx.x, lane = tid & 63, w = __builtin_amdgcn_readfirstlane(tid >> 6), b = blockIdx.x;
  unsigned pf0 = 0;
  if (tid < PF_LINES) pf0 = ((const GAS unsigned*)(__builtin_amdgcn_s_getpc() & ~255ull))[tid * 32];
  SCHED();
  const __amdgpu_buffer_rsrc_t rX = RSRC(p.x, 2048), rGW = RSRC(p.gw, 2048), rK = RSRC(p.K, 64 << 20), rV = RSRC(p.V, 64 << 20), rQ = RSRC(p.wq, 8 << 20), rO = RSRC(p.wo, 8 << 20),
                                rG = RSRC(p.wg, 7 << 20), rU = RSRC(p.wu, 7 << 20), rD = RSRC(p.wd, 7 << 20), rW = RSRC(p.ws, WS_END), rOut = RSRC(p.out, 2048);
  const int q_cg = b & 63, q_ks = b >> 6;
  const bool mlp = b < 224;
  const int g_cg = b % 28, g_ks = b / 28;
  const int hd = (b >> 3) & 3, sp = (b >> 5) * 8 + (b & 7);
  const int rg = (b & 7) + 8 * (b >> 5), cg4 = (b >> 3) & 3;

  const int qv = ((q_ks * 256 + w * 32 + (lane >> 3) * 4) * 4096 + q_cg * 64 + (lane & 7) * 8) * 2;
  v4u wqv[4];
#pragma unroll
  for (int n = 0; n < 4; ++n) wqv[n] = ld16<AUX_NT>(rQ, qv, n * 8192);
  SCHED();
  const v2u xq = ld8<0>(rX, (q_ks * 256 + w * 32 + (lane >> 3) * 4) * 2, 0);
  int vz = 0;
  PIN(vz);
  const unsigned E = ld4<AUX_SC1>(rW, vz, WS_EPOCH);
  const v2u xg = ld8<0>(rX, ((g_ks & 7) * 128 + w * 16 + (lane >> 4) * 4) * 2, 0);
  const unsigned xm = ld4<0>(rX, (w * 128 + lane * 2) * 2, 0), gm = ld4<0>(rGW, (w * 128 + lane * 2) * 2, 0);
  const int gv = mlp ? ((g_ks * 128 + w * 16 + (lane >> 4) * 4) * FF + g_cg * 128 + (lane & 15) * 8) * 2 : OOB;
  v4u gtv[4], upv[4];
  SCHED();
  asm volatile("s_barrier" ::: "memory");
#pragma unroll
  for (int n = 0; n < 4; ++n) gtv[n] = ld16<AUX_NT>(rG, gv, n * FF * 2);
  SCHED();
  {
    float acc[8] = {0.f, 0.f, 0.f, 0.f, 0.f, 0.f, 0.f, 0.f};
    fma8(acc, bflo(xq.x), wqv[0]); fma8(acc, bfhi(xq.x), wqv[1]); fma8(acc, bflo(xq.y), wqv[2]); fma8(acc, bfhi(xq.y), wqv[3]);
    const float r = reduce8(acc, lane);
    LDS1(L_RQ + (w * 64 + (lane & 7) * 8 + (lane >> 3)) * 4) = r;
  }
  SCHED();
#pragma unroll
  for (int n = 0; n < 4; ++n) upv[n] = ld16<AUX_NT>(rU, gv, n * FF * 2);
  SCHED();
  const unsigned T = E + 1u;
  Tags tg;
  tg.t0 = T & 1023u; tg.t1 = (T + (T >> 10)) & 1023u; tg.t2 = (T + (T >> 20)) & 1023u; tg.t3 = (T + (T >> 30) + 341u) & 1023u;
  {
    const float s = wave_sum(rbf(bflo(xm) * bflo(gm)) + rbf(bfhi(xm) * bfhi(gm)));
    if (lane == 0) LDS1(L_GM + w * 4) = s;
    if (tid == 0) *(LAS unsigned*)(L + L_CT) = 0u;
  }
  asm volatile("" :: "v"(pf0));
  BARRIER();
  if (w == 0) {
    f32x4 s = LDS4(L_RQ + (lane & 15) * 16);
#pragma unroll
    for (int ww = 1; ww < 8; ++ww) s += LDS4(L_RQ + ww * 256 + (lane & 15) * 16);
    st16(rW, lane < 16 ? WS_QP + (q_ks * 4096 + q_cg * 64 + lane * 4) * 4 : OOB, enc4(s, tg));
  }
  float gmix;
  {
    const f32x4 a = LDS4(L_GM), c = LDS4(L_GM + 16);
    const float sg = rbf(((a.x + a.y) + (a.z + a.w)) + ((c.x + c.y) + (c.z + c.w)));
    gmix = rbf(1.f / (1.f + __expf(-sg)));
  }
  const int kvb = hd * (16 << 20) + (sp * 8 + w) * 8192;
  constexpr int TSTEP = 512 * 8192;
  const int lv = lane * 16;
  v4u kt0[8], kt1[8], kt2[8];
  {
    float ag[8] = {0.f, 0.f, 0.f, 0.f, 0.f, 0.f, 0.f, 0.f}, au[8] = {0.f, 0.f, 0.f, 0.f, 0.f, 0.f, 0.f, 0.f};
    fma8(ag, bflo(xg.x), gtv[0]); fma8(ag, bfhi(xg.x), gtv[1]); fma8(ag, bflo(xg.y), gtv[2]); fma8(ag, bfhi(xg.y), gtv[3]);
    SCHED();
#pragma unroll
    for (int j = 0; j < 4; ++j) kt0[j] = ld16<AUX_NT>(rK, lv, kvb + j * 1024);
    SCHED();
    fma8(au, bflo(xg.x), upv[0]); fma8(au, bfhi(xg.x), upv[1]); fma8(au, bflo(xg.y), upv[2]); fma8(au, bfhi(xg.y), upv[3]);
    SCHED();
#pragma unroll
    for (int j = 4; j < 8; ++j) kt0[j] = ld16<AUX_NT>(rK, lv, kvb + j * 1024);
    SCHED();
    float t[4], u[2];
#pragma unroll
    for (int j = 0; j < 4; ++j) t[j] = swap_add32(ag[j], ag[j + 4]);
#pragma unroll
    for (int j = 0; j < 2; ++j) u[j] = swap_add16(t[j], t[j + 2]);
    *(LAS f32x2*)(L + L_RG + ((w * 2 + 0) * 128 + (lane & 15) * 8 + (lane >> 4) * 2) * 4) = f32x2{u[0], u[1]};
#pragma unroll
    for (int j = 0; j < 4; ++j) t[j] = swap_add32(au[j], au[j + 4]);
#pragma unroll
    for (int j = 0; j < 2; ++j) u[j] = swap_add16(t[j], t[j + 2]);
    *(LAS f32x2*)(L + L_RG + ((w * 2 + 1) * 128 + (lane & 15) * 8 + (lane >> 4) * 2) * 4) = f32x2{u[0], u[1]};
  }
  BARRIER();
  if (w == 1) {
    const int mat = lane >> 5, pc = lane & 31;
    f32x4 s = LDS4(L_RG + (mat * 128 + pc * 4) * 4);
#pragma unroll
    for (int ww = 1; ww < 8; ++ww) s += LDS4(L_RG + ((ww * 2 + mat) * 128 + pc * 4) * 4);
    st16(rW, mlp ? WS_GU + ((mat * 8 + g_ks) * FF + g_cg * 128 + pc * 4) * 4 : OOB, enc4(s, tg));
  }
  {
    const int qo = WS_QP + ((lane >> 5) * 2 * 4096 + (hd * 8 + w) * 128 + (lane & 31) * 4) * 4;
    v4u q0 = ld16<AUX_SC1>(rW, qo, 0), q1 = ld16<AUX_SC1>(rW, qo, 4096 * 4);
    SCHED();
#pragma unroll
    for (int j = 0; j < 8; ++j) kt1[j] = ld16<AUX_NT>(rK, lv, kvb + TSTEP + j * 1024);
#pragma unroll
    for (int j = 0; j < 8; ++j) kt2[j] = ld16<AUX_NT>(rK, lv, kvb + 2 * TSTEP + j * 1024);
    SCHED();
    if (!wave_ok(bad4(bad4(0u, q0, tg), q1, tg) & 1023u)) {
#pragma unroll 1
      for (unsigned spins = 0; spins < SPIN_CAP; ++spins) {
        __builtin_amdgcn_s_sleep(1);
        MEMFENCE();
        q0 = ld16<AUX_SC1>(rW, qo, 0); q1 = ld16<AUX_SC1>(rW, qo, 4096 * 4);
        if (wave_ok(bad4(bad4(0u, q0, tg), q1, tg) & 1023u)) break;
      }
    }
    const f32x4 a = dec4(q0) + dec4(q1);
    f32x4 s;
    s.x = swap_add32(a.x, a.x); s.y = swap_add32(a.y, a.y); s.z = swap_add32(a.z, a.z); s.w = swap_add32(a.w, a.w);
    if (lane < 32) *(LAS v2u*)(L + L_QS + (w * 128 + lane * 4) * 2) = v2u{pkbf(s.x, s.y), pkbf(s.z, s.w)};
  }
  BARRIER();

  const int g4 = lane >> 4, n16 = lane & 15;
  v4u qf[4];
#pragma unroll
  for (int s = 0; s < 4; ++s) qf[s] = LDSU4(L_QS + ((n16 & 7) * 128 + (4 * s + g4) * 8) * 2);
  f32x4 S[8];
  const int kb = L_KB + w * 8192;
  auto qk_tile = [&](const v4u (&kt)[8], int t) {
#pragma unroll
    for (int j = 0; j < 8; ++j) { const int r = 4 * g4 + (j & 3); LDSU4(kb + ((j >> 2) * 16 + r) * 256 + ((n16 ^ r) << 4)) = kt[j]; }
#pragma unroll
    for (int T2 = 0; T2 < 2; ++T2) {
      f32x4 c = {0.f, 0.f, 0.f, 0.f};
#pragma unroll
      for (int s = 0; s < 4; ++s) c = mfma(LDSU4(kb + (T2 * 16 + n16) * 256 + (((4 * s + g4) ^ n16) << 4)), qf[s], c);
      S[2 * t + T2] = c;
    }
  };
  v4u kt3[8], vt0[8], vt1[8], vt2[8], vt3[8];
  qk_tile(kt0, 0);
  SCHED();
#pragma unroll
  for (int j = 0; j < 8; ++j) kt3[j] = ld16<AUX_NT>(rK, lv, kvb + 3 * TSTEP + j * 1024);
  SCHED();
  qk_tile(kt1, 1);
  SCHED();
#pragma unroll
  for (int j = 0; j < 8; ++j) vt0[j] = ld16<AUX_NT>(rV, lv, kvb + j * 1024);
  SCHED();
  qk_tile(kt2, 2);
  SCHED();
#pragma unroll
  for (int j = 0; j < 8; ++j) vt1[j] = ld16<AUX_NT>(rV, lv, kvb + TSTEP + j * 1024);
  SCHED();
  qk_tile(kt3, 3);
  SCHED();
  float mw;
  {
    float mx = fmaxf(fmaxf(S[0].x, S[0].y), fmaxf(S[0].z, S[0].w));
#pragma unroll
    for (int i = 1; i < 8; ++i) mx = fmaxf(mx, fmaxf(fmaxf(S[i].x, S[i].y), fmaxf(S[i].z, S[i].w)));
    mw = xrow_max(mx) * SCL;
    if (lane < 8) LDS1(L_MS + (w * 8 + lane) * 4) = mw;
    ctr_add(L, lane, 1u);
  }
  float lsum = 0.f;
  v4u pf[4];
  auto soft_tile = [&](int t, float ref) {
    float e[8];
#pragma unroll
    for (int i = 0; i < 4; ++i) { e[i] = __builtin_amdgcn_exp2f(fmaf(S[2 * t][i], SCL, -ref)); e[4 + i] = __builtin_amdgcn_exp2f(fmaf(S[2 * t + 1][i], SCL, -ref)); }
    lsum += ((e[0] + e[1]) + (e[2] + e[3])) + ((e[4] + e[5]) + (e[6] + e[7]));
    pf[t] = v4u{pkbf(e[0], e[1]), pkbf(e[2], e[3]), pkbf(e[4], e[5]), pkbf(e[6], e[7])};
  };
  soft_tile(0, mw); soft_tile(1, mw);
  f32x4 O[8];
#pragma unroll
  for (int i = 0; i < 8; ++i) O[i] = f32x4{0.f, 0.f, 0.f, 0.f};
  auto pv_tile = [&](const v4u (&vt)[8], int t) {
#pragma unroll
    for (int i = 0; i < 8; ++i) {
      const unsigned sel = (i & 1) ? 0x07060302u : 0x05040100u;
      const v4u bf = {__builtin_amdgcn_perm(vt[1][i >> 1], vt[0][i >> 1], sel), __builtin_amdgcn_perm(vt[3][i >> 1], vt[2][i >> 1], sel),
                      __builtin_amdgcn_perm(vt[5][i >> 1], vt[4][i >> 1], sel), __builtin_amdgcn_perm(vt[7][i >> 1], vt[6][i >> 1], sel)};
      O[i] = mfma(pf[t], bf, O[i]);
    }
  };
  pv_tile(vt0, 0);
  SCHED();
#pragma unroll
  for (int j = 0; j < 8; ++j) vt2[j] = ld16<AUX_NT>(rV, lv, kvb + 2 * TSTEP + j * 1024);
  SCHED();
  pv_tile(vt1, 1);
  SCHED();
#pragma unroll
  for (int j = 0; j < 8; ++j) vt3[j] = ld16<AUX_NT>(rV, lv, kvb + 3 * TSTEP + j * 1024);
  SCHED();
  if (w == 0) {
    const int h_mat = lane / 28, h_kh = (lane % 28) / 14, h_pc = lane % 14;
    const int ho = lane < 56 ? WS_GU + ((h_mat * 8 + h_kh * 4) * FF + rg * 56 + h_pc * 4) * 4 : OOB;
    const unsigned hmask = lane < 56 ? 1023u : 0u;
    v4u hv[4];
#pragma unroll 1
    for (unsigned spins = 0; spins < SPIN_CAP; ++spins) {
      MEMFENCE();
#pragma unroll
      for (int i = 0; i < 4; ++i) hv[i] = ld16<AUX_SC1>(rW, ho, i * FF * 4);
      if (wave_ok(bad4(bad4(bad4(bad4(0u, hv[0], tg), hv[1], tg), hv[2], tg), hv[3], tg) & hmask)) break;
      __builtin_amdgcn_s_sleep(8);
    }
    const f32x4 s = (dec4(hv[0]) + dec4(hv[1])) + (dec4(hv[2]) + dec4(hv[3]));
    if (lane < 56) LDS4(L_GS + lane * 16) = s;
    MEMFENCE();
    float hval = 0.f;
    if (lane < 56) {
      const float gsum = rbf(LDS1(L_GS + lane * 4) + LDS1(L_GS + 14 * 16 + lane * 4));
      const float usum = rbf(LDS1(L_GS + 28 * 16 + lane * 4) + LDS1(L_GS + 42 * 16 + lane * 4));
      hval = rbf((gsum > 0.f ? gsum : 0.f) * usum) * rbf(1.f - gmix);
    }
    LDS1(L_HS + lane * 4) = hval;
  }
  SCHED();
  ctr_add(L, lane, 1u << 8);
  SCHED();
#pragma unroll 1
  for (unsigned spins = 0; spins < SPIN_CAP; ++spins) {
    if ((ctr_load(L) & 255u) >= 8u) break;
    __builtin_amdgcn_s_sleep(4);
  }
  MEMFENCE();
  {
    const float mcu = max_hi(LDS1(L_MS + (((lane >> 3) & 7) * 8 + (lane & 7)) * 4));
    const f32x4 mo = LDS4(L_MS + (w * 8 + (g4 & 1) * 4) * 4);
    f32x4 mn = LDS4(L_MS + ((g4 & 1) * 4) * 4);
#pragma unroll
    for (int ww = 1; ww < 8; ++ww) { const f32x4 t = LDS4(L_MS + (ww * 8 + (g4 & 1) * 4) * 4); mn = f32x4{fmaxf(mn.x, t.x), fmaxf(mn.y, t.y), fmaxf(mn.z, t.z), fmaxf(mn.w, t.w)}; }
    lsum *= __builtin_amdgcn_exp2f(mw - mcu);
    soft_tile(2, mcu); soft_tile(3, mcu);
    const f32x4 fo = {__builtin_amdgcn_exp2f(mo.x - mn.x), __builtin_amdgcn_exp2f(mo.y - mn.y), __builtin_amdgcn_exp2f(mo.z - mn.z), __builtin_amdgcn_exp2f(mo.w - mn.w)};
#pragma unroll
    for (int i = 0; i < 8; ++i) { O[i] *= fo; PIN(O[i]); }
    lsum = xrow_sum(lsum);
    if (lane < 8) { LDS1(L_LS + (w * 8 + lane) * 4) = lsum; LDS1(L_MC + lane * 4) = mcu; }
    PIN(pf[2]); PIN(pf[3]);
  }
  const int j4 = w & 3, hb = lane >> 5;
  const int hq = rg >> 1, half = rg & 1, s_ = (lane >> 3) * 8 + 2 * j4;
  int a_ow = LA(kb + ((g4 & 1) * 4 * 128 + n16 * 8 + (g4 >> 1) * 4) * 4);
  int a_or = LA(L_KB + ((2 * j4 + hb) * 128 + (lane & 31) * 4) * 4);
  int a_lr = LA(L_LS + ((lane & 7) * 8 + 2 * j4 + hb) * 4);
  int a_mc = LA(L_MC + (2 * j4 + hb) * 4);
  int a_op = WS_OP + ((((hd * 8 + 2 * j4 + hb) * 2 + ((lane & 31) >> 4)) * 64 + sp) * 64 + (lane & 15) * 4) * 4;
  int a_ml = (lane & 31) == 0 ? WS_ML + ((hd * 8 + 2 * j4 + hb) * 64 + sp) * 16 : OOB;
  int a_ma = WS_ML + (hq * 64 + lane) * 16;
  int a_mo = WS_ML + (hq * 64 + s_) * 16;
  int a_oo = WS_OP + (((hq * 2 + half) * 64 + s_) * 64 + (lane & 7) * 4) * 4;
  int a_rcw = LA(L_RC + (j4 * 64 + 4 * ((lane & 7) + 8 * (lane >> 5)) + ((lane >> 3) & 3)) * 4);
  int a_rcr = LA(L_RC + ((lane & 3) * 64 + j4 * 16 + hb * 8) * 4);
  int a_rfw = LA(L_RF + (j4 * 256 + (lane & 31) * 8 + hb * 4) * 4);
  int a_rfr = LA(L_RF + (g4 * 256 + j4 * 64 + n16 * 4) * 4);
  int a_fp = WS_FP + ((cg4 * 64 + rg) * 256 + j4 * 64 + n16 * 4 + g4) * 4;
  int a_hs = LA(L_HS + (j4 * 14 + hb * 7) * 4);
  unsigned tl_t = g4 == 0 ? tg.t0 : g4 == 1 ? tg.t1 : g4 == 2 ? tg.t2 : tg.t3;
  PIN(tl_t);
  PIN(a_ow); PIN(a_or); PIN(a_lr); PIN(a_mc); PIN(a_op); PIN(a_ml); PIN(a_ma); PIN(a_mo); PIN(a_oo); PIN(a_rcw); PIN(a_rcr); PIN(a_rfw); PIN(a_rfr); PIN(a_fp); PIN(a_hs);
  SCHED();
  if (w >= 4) __builtin_amdgcn_s_setprio(3);
  pv_tile(vt2, 2);
  SCHED();
  pv_tile(vt3, 3);
#pragma unroll
  for (int r = 0; r < 4; ++r) {
    const f32x4 v = g4 < 2 ? f32x4{O[0][r], O[1][r], O[2][r], O[3][r]} : f32x4{O[4][r], O[5][r], O[6][r], O[7][r]};
    AT4(a_ow + r * 512) = v;
  }
  ctr_add(L, lane, 1u << 16);
  if (w >= 4) {
#pragma unroll 1
    for (unsigned spins = 0; spins < SPIN_CAP; ++spins) { if (ctr_load(L) >= (8u << 16)) break; }
    MEMFENCE();
    {
      f32x4 part[8];
#pragma unroll
      for (int ww = 0; ww < 8; ++ww) part[ww] = AT4(a_or + ww * 8192);
      const float lw = AT1(a_lr), mh = AT1(a_mc);
      SCHED();
      const float Ls = sum8(lw);
      const f32x4 acc = ((part[0] + part[1]) + (part[2] + part[3])) + ((part[4] + part[5]) + (part[6] + part[7]));
      st16(rW, a_op, enc4(acc, tg));
      st16(rW, a_ml, v4u{__float_as_uint(mh), T, __float_as_uint(Ls), T});
    }
    SCHED();
    asm volatile("s_waitcnt vmcnt(0)" ::: "memory");
    v4u ma = ld16<AUX_SC1>(rW, a_ma, 0), m0 = ld16<AUX_SC1>(rW, a_mo, 0), m1 = ld16<AUX_SC1>(rW, a_mo, 16);
    v4u o0 = ld16<AUX_SC1>(rW, a_oo, 0), o1 = ld16<AUX_SC1>(rW, a_oo, 128), o2 = ld16<AUX_SC1>(rW, a_oo, 256), o3 = ld16<AUX_SC1>(rW, a_oo, 384);
    SCHED();
    ctr_add(L, lane, 1u << 24);
    SCHED();
    unsigned bad = ((((ma.y ^ T) | (ma.w ^ T)) | ((m0.y ^ T) | (m0.w ^ T))) | ((m1.y ^ T) | (m1.w ^ T))) | (bad4(bad4(bad4(bad4(0u, o0, tg), o1, tg), o2, tg), o3, tg) & 1023u);
    if (!wave_ok(bad)) {
#pragma unroll 1
      for (unsigned spins = 0; spins < SPIN_CAP; ++spins) {
        MEMFENCE();
        const bool again = bad != 0u;
        const int xa = again ? a_ma : OOB, xm2 = again ? a_mo : OOB, xo = again ? a_oo : OOB;
        const v4u na = ld16<AUX_SC1>(rW, xa, 0), n0 = ld16<AUX_SC1>(rW, xm2, 0), n1 = ld16<AUX_SC1>(rW, xm2, 16);
        const v4u p0 = ld16<AUX_SC1>(rW, xo, 0), p1 = ld16<AUX_SC1>(rW, xo, 128), p2 = ld16<AUX_SC1>(rW, xo, 256), p3 = ld16<AUX_SC1>(rW, xo, 384);
        ma = again ? na : ma; m0 = again ? n0 : m0; m1 = again ? n1 : m1; o0 = again ? p0 : o0; o1 = again ? p1 : o1; o2 = again ? p2 : o2; o3 = again ? p3 : o3;
        bad = ((((ma.y ^ T) | (ma.w ^ T)) | ((m0.y ^ T) | (m0.w ^ T))) | ((m1.y ^ T) | (m1.w ^ T))) | (bad4(bad4(bad4(bad4(0u, o0, tg), o1, tg), o2, tg), o3, tg) & 1023u);
        if (wave_ok(bad)) break;
      }
    }
    const float M = wave_max(__uint_as_float(ma.x));
    const float inv = __builtin_amdgcn_rcpf(wave_sum(__builtin_amdgcn_exp2f(__uint_as_float(ma.x) - M) * __uint_as_float(ma.z)));
    const float w0 = __builtin_amdgcn_exp2f(__uint_as_float(m0.x) - M) * inv, w1 = __builtin_amdgcn_exp2f(__uint_as_float(m1.x) - M) * inv;
    const f32x4 a0 = dec4(o0) * w0 + dec4(o2) * w1, a1 = dec4(o1) * w0 + dec4(o3) * w1;
    const float vals[8] = {a0.x, a0.y, a0.z, a0.w, a1.x, a1.y, a1.z, a1.w};
    AT1(a_rcw) = reduce8(vals, lane);
    BARRIER();
  } else {
    const int ov = ((rg * 64 + j4 * 16 + hb * 8) * 1024 + cg4 * 256 + (lane & 31) * 8) * 2;
    const int dv = ((rg * 56 + j4 * 14 + hb * 7) * 1024 + cg4 * 256 + (lane & 31) * 8) * 2;
#pragma unroll 1
    for (unsigned spins = 0; spins < SPIN_CAP; ++spins) {
      if (((ctr_load(L) >> 8) & 255u) >= 8u) break;
      __builtin_amdgcn_s_sleep(4);
    }
    MEMFENCE();
    v4u wdv[7], wov[8];
#pragma unroll
    for (int n = 0; n < 7; ++n) wdv[n] = ld16<AUX_NT>(rD, dv, n * 2048);
#pragma unroll
    for (int n = 0; n < 4; ++n) wov[n] = ld16<AUX_NT>(rO, ov, n * 2048);
    SCHED();
    float accd[4];
    {
      float acc[8] = {0.f, 0.f, 0.f, 0.f, 0.f, 0.f, 0.f, 0.f};
#pragma unroll
      for (int n = 0; n < 7; ++n) fma8(acc, AT1(a_hs + n * 4), wdv[n]);
#pragma unroll
      for (int j = 0; j < 4; ++j) { accd[j] = swap_add32(acc[j], acc[j + 4]); PIN(accd[j]); }
    }
    SCHED();
#pragma unroll 1
    for (unsigned spins = 0; spins < SPIN_CAP; ++spins) { if ((ctr_load(L) >> 24) >= 4u) break; __builtin_amdgcn_s_sleep(1); }
    MEMFENCE();
#pragma unroll
    for (int n = 4; n < 8; ++n) wov[n] = ld16<AUX_NT>(rO, ov, n * 2048);
    SCHED();
    unsigned wp[4][8];
#pragma unroll
    for (int pr = 0; pr < 2; ++pr)
#pragma unroll
      for (int j = 0; j < 4; ++j) { wp[pr][2 * j] = __builtin_amdgcn_perm(wov[2 * pr + 1][j], wov[2 * pr][j], 0x05040100u); wp[pr][2 * j + 1] = __builtin_amdgcn_perm(wov[2 * pr + 1][j], wov[2 * pr][j], 0x07060302u); PIN(wp[pr][2 * j]); PIN(wp[pr][2 * j + 1]); }
    float ao[8] = {0.f, 0.f, 0.f, 0.f, 0.f, 0.f, 0.f, 0.f};
#pragma unroll
    for (int j = 0; j < 8; ++j) PIN(ao[j]);
    BARRIER();
    {
      const f32x4 pa = AT4(a_rcr), pb = AT4(a_rcr + 16);
      float o[8] = {pa.x, pa.y, pa.z, pa.w, pb.x, pb.y, pb.z, pb.w};
      sum4x8(o);
#pragma unroll
      for (int pr = 0; pr < 4; ++pr) {
        const unsigned op = pkbf(o[2 * pr], o[2 * pr + 1]);
        if (pr >= 2) {
#pragma unroll
          for (int j = 0; j < 4; ++j) { wp[pr][2 * j] = __builtin_amdgcn_perm(wov[2 * pr + 1][j], wov[2 * pr][j], 0x05040100u); wp[pr][2 * j + 1] = __builtin_amdgcn_perm(wov[2 * pr + 1][j], wov[2 * pr][j], 0x07060302u); }
        }
#pragma unroll
        for (int j = 0; j < 8; ++j) ao[j] = dot2(wp[pr][j], op, ao[j]);
      }
      f32x4 v;
      v.x = fmaf(gmix, swap_add32(ao[0], ao[4]), accd[0]); v.y = fmaf(gmix, swap_add32(ao[1], ao[5]), accd[1]);
      v.z = fmaf(gmix, swap_add32(ao[2], ao[6]), accd[2]); v.w = fmaf(gmix, swap_add32(ao[3], ao[7]), accd[3]);
      AT4(a_rfw) = v;
    }
  }
  if (w >= 4) return;
  BARRIER();
  {
    const f32x4 pc = AT4(a_rfr);
    const float s = swap_add16(swap_add32(pc.x, pc.z), swap_add32(pc.y, pc.w));
    __builtin_amdgcn_raw_buffer_store_b32(enc1(s, tl_t), rW, a_fp, 0, AUX_SC1);
  }
  if (b < 32 && w <= 1) {
    if (w == 1) __builtin_amdgcn_s_sleep(5);
    const int c = b >> 3, seg = b & 7;
    const int fo = WS_FP + ((c * 64 + (lane >> 3)) * 256 + seg * 32 + (lane & 7) * 4) * 4;
    const int oo = (lane & 8) ? OOB : (c * 256 + seg * 32 + (lane & 7) * 4 + (lane >> 4)) * 2;
    v4u f[8];
    asm volatile("s_waitcnt vmcnt(0)" ::: "memory");
#pragma unroll 1
    for (unsigned spins = 0; spins < SPIN_CAP; ++spins) {
      MEMFENCE();
#pragma unroll
      for (int n = 0; n < 8; ++n) f[n] = ld16<AUX_SC1>(rW, fo, n * 8192);
      unsigned bad = 0;
#pragma unroll
      for (int n = 0; n < 8; ++n) bad = bad4(bad, f[n], tg);
      if (wave_ok(bad & 1023u)) break;
    }
    if (b == 0 && w == 0) __builtin_amdgcn_raw_buffer_store_b32(T, rW, lane == 0 ? WS_EPOCH : OOB, 0, AUX_SC1);
    const f32x4 d = ((dec4(f[0]) + dec4(f[1])) + (dec4(f[2]) + dec4(f[3]))) + ((dec4(f[4]) + dec4(f[5])) + (dec4(f[6]) + dec4(f[7])));
    const float r = reduce4(d);
    __builtin_amdgcn_raw_buffer_store_b16((unsigned short)pkbf(r, r), rOut, oo, 0, 0);
  }
}

}

extern "C" void kernel_launch(void* const* d_in, const int* in_sizes, int n_in, void* d_out, int out_size, void* d_ws, size_t ws_size, hipStream_t stream) {
  fk::Params p;
  p.x = d_in[0]; p.K = d_in[1]; p.V = d_in[2]; p.wq = d_in[3]; p.wo = d_in[4];
  p.wg = d_in[6]; p.wu = d_in[7]; p.wd = d_in[8]; p.gw = d_in[9];
  p.out = d_out; p.ws = (unsigned char*)d_ws;
  (void)hipFuncSetAttribute((const void*)fk::fused_kernel, hipFuncAttributeMaxDynamicSharedMemorySize, fk::LDS_BYTES);
  hipLaunchKernelGGL(fk::fused_kernel, dim3(fk::NWG), dim3(fk::NTHR), fk::LDS_BYTES, stream, p);
}
```

```cpp
#include <hip/hip_runtime.h>
#include <stdint.h>

namespace fk {

typedef unsigned v4u __attribute__((ext_vector_type(4)));
typedef unsigned v2u __attribute__((ext_vector_type(2)));
typedef float f32x4 __attribute__((ext_vector_type(4)));
typedef float f32x2 __attribute__((ext_vector_type(2)));
typedef __bf16 bf16x8 __attribute__((ext_vector_type(8)));
typedef __bf16 bf16x2 __attribute__((ext_vector_type(2)));
#define LAS __attribute__((address_space(3)))
#define GAS __attribute__((address_space(1)))

constexpr int NWG = 256, NTHR = 512;
constexpr int FF = 3584;
constexpr int WS_EPOCH = 0;
constexpr int WS_QP = 4096;
constexpr int WS_GU = WS_QP + 4 * 4096 * 4;
constexpr int WS_ML = WS_GU + 2 * 8 * FF * 4;
constexpr int WS_OP = WS_ML + 32 * 64 * 16;
constexpr int WS_FP = WS_OP + 32 * 2 * 64 * 64 * 4;
constexpr int WS_END = WS_FP + 4 * 64 * 256 * 4;
constexpr int L_KB = 0;
constexpr int L_RQ = 65536;
constexpr int L_RG = L_RQ + 2048;
constexpr int L_RC = L_RG + 8192;
constexpr int L_RF = L_RC + 2048;
constexpr int L_QS = L_RF + 8192;
constexpr int L_MS = L_QS + 2048;
constexpr int L_LS = L_MS + 256;
constexpr int L_MC = L_LS + 256;
constexpr int L_CT = L_MC + 32;
constexpr int L_GS = L_CT + 32;
constexpr int L_HS = L_GS + 1024;
constexpr int L_GM = L_HS + 256;
constexpr int L_END = L_GM + 32;
constexpr int LDS_BYTES = 90112;
static_assert(L_END <= LDS_BYTES, "lds");

constexpr int AUX_NT = 2, AUX_SC1 = 16;
constexpr int OOB = 0x7ffffff0;
constexpr unsigned SPIN_CAP = 1u << 18;
constexpr float SCL = 0.08838834764831845f * 1.4426950408889634f;
constexpr int PF_LINES = 125;

struct Params {
  const void *x, *K, *V, *wq, *wo, *wg, *wu, *wd, *gw;
  void* out;
  unsigned char* ws;
};

__device__ __forceinline__ float bflo(unsigned u) { return __uint_as_float(u << 16); }
__device__ __forceinline__ float bfhi(unsigned u) { return __uint_as_float(u & 0xffff0000u); }
__device__ __forceinline__ unsigned pkbf(float a, float b) { f32x2 v = {a, b}; return __builtin_bit_cast(unsigned, __builtin_convertvector(v, bf16x2)); }
__device__ __forceinline__ float rbf(float a) { return bflo(pkbf(a, 0.f)); }
template <int AUX> __device__ __forceinline__ v4u ld16(__amdgpu_buffer_rsrc_t rs, int voff, int soff) { return __builtin_bit_cast(v4u, __builtin_amdgcn_raw_buffer_load_b128(rs, voff, soff, AUX)); }
template <int AUX> __device__ __forceinline__ v2u ld8(__amdgpu_buffer_rsrc_t rs, int voff, int soff) { return __builtin_bit_cast(v2u, __builtin_amdgcn_raw_buffer_load_b64(rs, voff, soff, AUX)); }
template <int AUX> __device__ __forceinline__ unsigned ld4(__amdgpu_buffer_rsrc_t rs, int voff, int soff) { return __builtin_amdgcn_raw_buffer_load_b32(rs, voff, soff, AUX); }
__device__ __forceinline__ void st16(__amdgpu_buffer_rsrc_t rs, int voff, v4u v) { __builtin_amdgcn_raw_buffer_store_b128(v, rs, voff, 0, AUX_SC1); }
#define RSRC(p, n) __builtin_amdgcn_make_buffer_rsrc((void*)(p), 0, (n), 0x00020000)
#define SCHED() __builtin_amdgcn_sched_barrier(0)
#define BARRIER() asm volatile("s_waitcnt lgkmcnt(0)\n\ts_barrier" ::: "memory")
#define MEMFENCE() asm volatile("" ::: "memory")
#define PIN(x) asm volatile("" : "+v"(x))

__device__ __forceinline__ float swap_add32(float a, float b) {
  auto s = __builtin_amdgcn_permlane32_swap(__float_as_uint(a), __float_as_uint(b), false, false);
  return __uint_as_float(s[0]) + __uint_as_float(s[1]);
}
__device__ __forceinline__ float swap_add16(float a, float b) {
  auto s = __builtin_amdgcn_permlane16_swap(__float_as_uint(a), __float_as_uint(b), false, false);
  return __uint_as_float(s[0]) + __uint_as_float(s[1]);
}
template <int CTRL> __device__ __forceinline__ float dpp(float x) { return __builtin_bit_cast(float, __builtin_amdgcn_mov_dpp(__builtin_bit_cast(int, x), CTRL, 0xf, 0xf, true)); }
constexpr int ROR8 = 0x128, ROR4 = 0x124, ROR2 = 0x122, ROR1 = 0x121;
constexpr int XOR1 = 0xB1, XOR2 = 0x4E, HMIR = 0x141;
__device__ __forceinline__ float xrow_max(float x) {
  auto s = __builtin_amdgcn_permlane16_swap(__float_as_uint(x), __float_as_uint(x), false, false);
  x = fmaxf(__uint_as_float(s[0]), __uint_as_float(s[1]));
  auto t = __builtin_amdgcn_permlane32_swap(__float_as_uint(x), __float_as_uint(x), false, false);
  return fmaxf(__uint_as_float(t[0]), __uint_as_float(t[1]));
}
__device__ __forceinline__ float xrow_sum(float x) {
  auto s = __builtin_amdgcn_permlane16_swap(__float_as_uint(x), __float_as_uint(x), false, false);
  x = __uint_as_float(s[0]) + __uint_as_float(s[1]);
  auto t = __builtin_amdgcn_permlane32_swap(__float_as_uint(x), __float_as_uint(x), false, false);
  return __uint_as_float(t[0]) + __uint_as_float(t[1]);
}
__device__ __forceinline__ float sum8(float x) { x += dpp<XOR1>(x); x += dpp<XOR2>(x); return x + dpp<HMIR>(x); }
__device__ __forceinline__ float sum_hi(float x) { x += dpp<ROR8>(x); return xrow_sum(x); }
__device__ __forceinline__ float max_hi(float x) { x = fmaxf(x, dpp<ROR8>(x)); return xrow_max(x); }
__device__ __forceinline__ float wave_sum(float x) { x += dpp<ROR4>(x); x += dpp<ROR2>(x); x += dpp<ROR1>(x); return sum_hi(x); }
__device__ __forceinline__ float wave_max(float x) { x = fmaxf(x, dpp<ROR4>(x)); x = fmaxf(x, dpp<ROR2>(x)); x = fmaxf(x, dpp<ROR1>(x)); return max_hi(x); }
__device__ __forceinline__ void sum4x8(float (&o)[8]) {
#define DPP8(M) "v_add_f32_dpp %0, %0, %0 " M " row_mask:0xf bank_mask:0xf\n\tv_add_f32_dpp %1, %1, %1 " M " row_mask:0xf bank_mask:0xf\n\tv_add_f32_dpp %2, %2, %2 " M " row_mask:0xf bank_mask:0xf\n\t" \
                "v_add_f32_dpp %3, %3, %3 " M " row_mask:0xf bank_mask:0xf\n\tv_add_f32_dpp %4, %4, %4 " M " row_mask:0xf bank_mask:0xf\n\tv_add_f32_dpp %5, %5, %5 " M " row_mask:0xf bank_mask:0xf\n\t" \
                "v_add_f32_dpp %6, %6, %6 " M " row_mask:0xf bank_mask:0xf\n\tv_add_f32_dpp %7, %7, %7 " M " row_mask:0xf bank_mask:0xf\n\t"
  asm volatile("s_nop 1\n\t" DPP8("quad_perm:[1,0,3,2]") DPP8("quad_perm:[2,3,0,1]") "s_nop 1" : "+v"(o[0]), "+v"(o[1]), "+v"(o[2]), "+v"(o[3]), "+v"(o[4]), "+v"(o[5]), "+v"(o[6]), "+v"(o[7]));
#undef DPP8
}
__device__ __forceinline__ float reduce4(f32x4 v) {
  const float x = swap_add16(swap_add32(v.x, v.z), swap_add32(v.y, v.w));
  return x + dpp<ROR8>(x);
}
__device__ __forceinline__ float reduce8(const float (&a)[8], int lane) {
  float t[4], u[2];
#pragma unroll
  for (int j = 0; j < 4; ++j) t[j] = swap_add32(a[j], a[j + 4]);
#pragma unroll
  for (int j = 0; j < 2; ++j) u[j] = swap_add16(t[j], t[j + 2]);
  const float v0 = u[0] + dpp<ROR8>(u[0]), v1 = u[1] + dpp<ROR8>(u[1]);
  return (lane & 8) ? v1 : v0;
}
__device__ __forceinline__ void fma8(float (&acc)[8], float xv, v4u w) {
#pragma unroll
  for (int j = 0; j < 4; ++j) { acc[2 * j] = fmaf(xv, bflo(w[j]), acc[2 * j]); acc[2 * j + 1] = fmaf(xv, bfhi(w[j]), acc[2 * j + 1]); }
}
__device__ __forceinline__ float dot2(unsigned a, unsigned b, float c) { return __builtin_amdgcn_fdot2_f32_bf16(__builtin_bit_cast(bf16x2, a), __builtin_bit_cast(bf16x2, b), c, false); }
__device__ __forceinline__ f32x4 mfma(v4u a, v4u b, f32x4 c) { return __builtin_amdgcn_mfma_f32_16x16x32_bf16(__builtin_bit_cast(bf16x8, a), __builtin_bit_cast(bf16x8, b), c, 0, 0, 0); }

struct Tags { unsigned t0, t1, t2, t3; };
__device__ __forceinline__ unsigned enc1(float v, unsigned t) { return ((__float_as_uint(v) + 512u) & ~1023u) | t; }
__device__ __forceinline__ v4u enc4(f32x4 v, const Tags& t) { return v4u{enc1(v.x, t.t0), enc1(v.y, t.t1), enc1(v.z, t.t2), enc1(v.w, t.t3)}; }
__device__ __forceinline__ unsigned orx(unsigned acc, unsigned x, unsigned t) { return __builtin_amdgcn_bitop3_b32(acc, x, t, 0xF6); }
__device__ __forceinline__ unsigned bad4(unsigned acc, v4u v, const Tags& t) { return orx(orx(orx(orx(acc, v.x, t.t0), v.y, t.t1), v.z, t.t2), v.w, t.t3); }
__device__ __forceinline__ f32x4 dec4(v4u v) { return __builtin_bit_cast(f32x4, v & ~1023u); }
__device__ __forceinline__ bool wave_ok(unsigned bad) { return __builtin_amdgcn_ballot_w64(bad != 0u) == 0ull; }

__device__ __forceinline__ void ctr_add(LAS unsigned char* L, int lane, unsigned v) {
  if (lane == 0) asm volatile("ds_add_u32 %0, %1" :: "v"((unsigned)(size_t)(L + L_CT)), "v"(v) : "memory");
}
__device__ __forceinline__ unsigned ctr_load(LAS unsigned char* L) { return __builtin_amdgcn_readfirstlane(__hip_atomic_load((LAS unsigned*)(L + L_CT), __ATOMIC_RELAXED, __HIP_MEMORY_SCOPE_WORKGROUP)); }
#define LDS4(off) (*(LAS f32x4*)(L + (off)))
#define LDSU4(off) (*(LAS v4u*)(L + (off)))
#define LDS1(off) (*(LAS float*)(L + (off)))
#define LA(off) ((int)(unsigned)(size_t)(L + (off)))
#define AT4(a) (*(LAS f32x4*)(size_t)(unsigned)(a))
#define AT1(a) (*(LAS float*)(size_t)(unsigned)(a))

__global__ void __launch_bounds__(NTHR, 2) fused_kernel(Params p) {
  extern __shared__ __attribute__((aligned(16))) unsigned char smem[];
  LAS unsigned char* L = (LAS unsigned char*)smem;
  const int tid = threadIdx.x, lane = tid & 63, w = __builtin_amdgcn_readfirstlane(tid >> 6), b = blockIdx.x;
  unsigned pf0 = 0;
  if (tid < PF_LINES) pf0 = ((const GAS unsigned*)(__builtin_amdgcn_s_getpc() & ~255ull))[tid * 32];
  SCHED();
  const __amdgpu_buffer_rsrc_t rX = RSRC(p.x, 2048), rGW = RSRC(p.gw, 2048), rK = RSRC(p.K, 64 << 20), rV = RSRC(p.V, 64 << 20), rQ = RSRC(p.wq, 8 << 20), rO = RSRC(p.wo, 8 << 20),
                                rG = RSRC(p.wg, 7 << 20), rU = RSRC(p.wu, 7 << 20), rD = RSRC(p.wd, 7 << 20), rW = RSRC(p.ws, WS_END), rOut = RSRC(p.out, 2048);
  const int q_cg = b & 63, q_ks = b >> 6;
  const bool mlp = b < 224;
  const int g_cg = b % 28, g_ks = b / 28;
  const int hd = (b >> 3) & 3, sp = (b >> 5) * 8 + (b & 7);
  const int rg = (b & 7) + 8 * (b >> 5), cg4 = (b >> 3) & 3;

  const int qv = ((q_ks * 256 + w * 32 + (lane >> 3) * 4) * 4096 + q_cg * 64 + (lane & 7) * 8) * 2;
  v4u wqv[4];
#pragma unroll
  for (int n = 0; n < 4; ++n) wqv[n] = ld16<AUX_NT>(rQ, qv, n * 8192);
  SCHED();
  const v2u xq = ld8<0>(rX, (q_ks * 256 + w * 32 + (lane >> 3) * 4) * 2, 0);
  int vz = 0;
  PIN(vz);
  const unsigned E = ld4<AUX_SC1>(rW, vz, WS_EPOCH);
  const v2u xg = ld8<0>(rX, ((g_ks & 7) * 128 + w * 16 + (lane >> 4) * 4) * 2, 0);
  const unsigned xm = ld4<0>(rX, (w * 128 + lane * 2) * 2, 0), gm = ld4<0>(rGW, (w * 128 + lane * 2) * 2, 0);
  const int gv = mlp ? ((g_ks * 128 + w * 16 + (lane >> 4) * 4) * FF + g_cg * 128 + (lane & 15) * 8) * 2 : OOB;
  v4u gtv[4], upv[4];
  SCHED();
  asm volatile("s_barrier" ::: "memory");
#pragma unroll
  for (int n = 0; n < 4; ++n) gtv[n] = ld16<AUX_NT>(rG, gv, n * FF * 2);
  SCHED();
  {
    float acc[8] = {0.f, 0.f, 0.f, 0.f, 0.f, 0.f, 0.f, 0.f};
    fma8(acc, bflo(xq.x), wqv[0]); fma8(acc, bfhi(xq.x), wqv[1]); fma8(acc, bflo(xq.y), wqv[2]); fma8(acc, bfhi(xq.y), wqv[3]);
    const float r = reduce8(acc, lane);
    LDS1(L_RQ + (w * 64 + (lane & 7) * 8 + (lane >> 3)) * 4) = r;
  }
  SCHED();
#pragma unroll
  for (int n = 0; n < 4; ++n) upv[n] = ld16<AUX_NT>(rU, gv, n * FF * 2);
  SCHED();
  const unsigned T = E + 1u;
  Tags tg;
  tg.t0 = T & 1023u; tg.t1 = (T + (T >> 10)) & 1023u; tg.t2 = (T + (T >> 20)) & 1023u; tg.t3 = (T + (T >> 30) + 341u) & 1023u;
  {
    const float s = wave_sum(rbf(bflo(xm) * bflo(gm)) + rbf(bfhi(xm) * bfhi(gm)));
    if (lane == 0) LDS1(L_GM + w * 4) = s;
    if (tid == 0) *(LAS unsigned*)(L + L_CT) = 0u;
  }
  asm volatile("" :: "v"(pf0));
  BARRIER();
  if (w == 0) {
    f32x4 s = LDS4(L_RQ + (lane & 15) * 16);
#pragma unroll
    for (int ww = 1; ww < 8; ++ww) s += LDS4(L_RQ + ww * 256 + (lane & 15) * 16);
    st16(rW, lane < 16 ? WS_QP + (q_ks * 4096 + q_cg * 64 + lane * 4) * 4 : OOB, enc4(s, tg));
  }
  float gmix;
  {
    const f32x4 a = LDS4(L_GM), c = LDS4(L_GM + 16);
    const float sg = rbf(((a.x + a.y) + (a.z + a.w)) + ((c.x + c.y) + (c.z + c.w)));
    gmix = rbf(1.f / (1.f + __expf(-sg)));
  }
  const int kvb = hd * (16 << 20) + (sp * 8 + w) * 8192;
  constexpr int TSTEP = 512 * 8192;
  const int lv = lane * 16;
  v4u kt0[8], kt1[8], kt2[8];
  {
    float ag[8] = {0.f, 0.f, 0.f, 0.f, 0.f, 0.f, 0.f, 0.f}, au[8] = {0.f, 0.f, 0.f, 0.f, 0.f, 0.f, 0.f, 0.f};
    fma8(ag, bflo(xg.x), gtv[0]); fma8(ag, bfhi(xg.x), gtv[1]); fma8(ag, bflo(xg.y), gtv[2]); fma8(ag, bfhi(xg.y), gtv[3]);
    SCHED();
#pragma unroll
    for (int j = 0; j < 4; ++j) kt0[j] = ld16<AUX_NT>(rK, lv, kvb + j * 1024);
    SCHED();
    fma8(au, bflo(xg.x), upv[0]); fma8(au, bfhi(xg.x), upv[1]); fma8(au, bflo(xg.y), upv[2]); fma8(au, bfhi(xg.y), upv[3]);
    SCHED();
#pragma unroll
    for (int j = 4; j < 8; ++j) kt0[j] = ld16<AUX_NT>(rK, lv, kvb + j * 1024);
    SCHED();
    float t[4], u[2];
#pragma unroll
    for (int j = 0; j < 4; ++j) t[j] = swap_add32(ag[j], ag[j + 4]);
#pragma unroll
    for (int j = 0; j < 2; ++j) u[j] = swap_add16(t[j], t[j + 2]);
    *(LAS f32x2*)(L + L_RG + ((w * 2 + 0) * 128 + (lane & 15) * 8 + (lane >> 4) * 2) * 4) = f32x2{u[0], u[1]};
#pragma unroll
    for (int j = 0; j < 4; ++j) t[j] = swap_add32(au[j], au[j + 4]);
#pragma unroll
    for (int j = 0; j < 2; ++j) u[j] = swap_add16(t[j], t[j + 2]);
    *(LAS f32x2*)(L + L_RG + ((w * 2 + 1) * 128 + (lane & 15) * 8 + (lane >> 4) * 2) * 4) = f32x2{u[0], u[1]};
  }
  BARRIER();
  if (w == 1) {
    const int mat = lane >> 5, pc = lane & 31;
    f32x4 s = LDS4(L_RG + (mat * 128 + pc * 4) * 4);
#pragma unroll
    for (int ww = 1; ww < 8; ++ww) s += LDS4(L_RG + ((ww * 2 + mat) * 128 + pc * 4) * 4);
    st16(rW, mlp ? WS_GU + ((mat * 8 + g_ks) * FF + g_cg * 128 + pc * 4) * 4 : OOB, enc4(s, tg));
  }
  {
    const int qo = WS_QP + ((lane >> 5) * 2 * 4096 + (hd * 8 + w) * 128 + (lane & 31) * 4) * 4;
    v4u q0 = ld16<AUX_SC1>(rW, qo, 0), q1 = ld16<AUX_SC1>(rW, qo, 4096 * 4);
    SCHED();
#pragma unroll
    for (int j = 0; j < 8; ++j) kt1[j] = ld16<AUX_NT>(rK, lv, kvb + TSTEP + j * 1024);
#pragma unroll
    for (int j = 0; j < 8; ++j) kt2[j] = ld16<AUX_NT>(rK, lv, kvb + 2 * TSTEP + j * 1024);
    SCHED();
    if (!wave_ok(bad4(bad4(0u, q0, tg), q1, tg) & 1023u)) {
#pragma unroll 1
      for (unsigned spins = 0; spins < SPIN_CAP; ++spins) {
        __builtin_amdgcn_s_sleep(1);
        MEMFENCE();
        q0 = ld16<AUX_SC1>(rW, qo, 0); q1 = ld16<AUX_SC1>(rW, qo, 4096 * 4);
        if (wave_ok(bad4(bad4(0u, q0, tg), q1, tg) & 1023u)) break;
      }
    }
    const f32x4 a = dec4(q0) + dec4(q1);
    f32x4 s;
    s.x = swap_add32(a.x, a.x); s.y = swap_add32(a.y, a.y); s.z = swap_add32(a.z, a.z); s.w = swap_add32(a.w, a.w);
    if (lane < 32) *(LAS v2u*)(L + L_QS + (w * 128 + lane * 4) * 2) = v2u{pkbf(s.x, s.y), pkbf(s.z, s.w)};
  }
  BARRIER();

  const int g4 = lane >> 4, n16 = lane & 15;
  v4u qf[4];
#pragma unroll
  for (int s = 0; s < 4; ++s) qf[s] = LDSU4(L_QS + ((n16 & 7) * 128 + (4 * s + g4) * 8) * 2);
  f32x4 S[8];
  const int kb = L_KB + w * 8192;
  auto qk_tile = [&](const v4u (&kt)[8], int t) {
#pragma unroll
    for (int j = 0; j < 8; ++j) { const int r = 4 * g4 + (j & 3); LDSU4(kb + ((j >> 2) * 16 + r) * 256 + ((n16 ^ r) << 4)) = kt[j]; }
#pragma unroll
    for (int T2 = 0; T2 < 2; ++T2) {
      f32x4 c = {0.f, 0.f, 0.f, 0.f};
#pragma unroll
      for (int s = 0; s < 4; ++s) c = mfma(LDSU4(kb + (T2 * 16 + n16) * 256 + (((4 * s + g4) ^ n16) << 4)), qf[s], c);
      S[2 * t + T2] = c;
    }
  };
  v4u kt3[8], vt0[8], vt1[8], vt2[8], vt3[8];
  qk_tile(kt0, 0);
  SCHED();
#pragma unroll
  for (int j = 0; j < 8; ++j) kt3[j] = ld16<AUX_NT>(rK, lv, kvb + 3 * TSTEP + j * 1024);
  SCHED();
  qk_tile(kt1, 1);
  SCHED();
#pragma unroll
  for (int j = 0; j < 8; ++j) vt0[j] = ld16<AUX_NT>(rV, lv, kvb + j * 1024);
  SCHED();
  qk_tile(kt2, 2);
  SCHED();
#pragma unroll
  for (int j = 0; j < 8; ++j) vt1[j] = ld16<AUX_NT>(rV, lv, kvb + TSTEP + j * 1024);
  SCHED();
  qk_tile(kt3, 3);
  SCHED();
  float mw;
  {
    float mx = fmaxf(fmaxf(S[0].x, S[0].y), fmaxf(S[0].z, S[0].w));
#pragma unroll
    for (int i = 1; i < 8; ++i) mx = fmaxf(mx, fmaxf(fmaxf(S[i].x, S[i].y), fmaxf(S[i].z, S[i].w)));
    mw = xrow_max(mx) * SCL;
    if (lane < 8) LDS1(L_MS + (w * 8 + lane) * 4) = mw;
    ctr_add(L, lane, 1u);
  }
  float lsum = 0.f;
  v4u pf[4];
  auto soft_tile = [&](int t, float ref) {
    float e[8];
#pragma unroll
    for (int i = 0; i < 4; ++i) { e[i] = __builtin_amdgcn_exp2f(fmaf(S[2 * t][i], SCL, -ref)); e[4 + i] = __builtin_amdgcn_exp2f(fmaf(S[2 * t + 1][i], SCL, -ref)); }
    lsum += ((e[0] + e[1]) + (e[2] + e[3])) + ((e[4] + e[5]) + (e[6] + e[7]));
    pf[t] = v4u{pkbf(e[0], e[1]), pkbf(e[2], e[3]), pkbf(e[4], e[5]), pkbf(e[6], e[7])};
  };
  soft_tile(0, mw); soft_tile(1, mw);
  f32x4 O[8];
#pragma unroll
  for (int i = 0; i < 8; ++i) O[i] = f32x4{0.f, 0.f, 0.f, 0.f};
  auto pv_tile = [&](const v4u (&vt)[8], int t) {
#pragma unroll
    for (int i = 0; i < 8; ++i) {
      const unsigned sel = (i & 1) ? 0x07060302u : 0x05040100u;
      const v4u bf = {__builtin_amdgcn_perm(vt[1][i >> 1], vt[0][i >> 1], sel), __builtin_amdgcn_perm(vt[3][i >> 1], vt[2][i >> 1], sel),
                      __builtin_amdgcn_perm(vt[5][i >> 1], vt[4][i >> 1], sel), __builtin_amdgcn_perm(vt[7][i >> 1], vt[6][i >> 1], sel)};
      O[i] = mfma(pf[t], bf, O[i]);
    }
  };
  pv_tile(vt0, 0);
  SCHED();
#pragma unroll
  for (int j = 0; j < 8; ++j) vt2[j] = ld16<AUX_NT>(rV, lv, kvb + 2 * TSTEP + j * 1024);
  SCHED();
  pv_tile(vt1, 1);
  SCHED();
#pragma unroll
  for (int j = 0; j < 8; ++j) vt3[j] = ld16<AUX_NT>(rV, lv, kvb + 3 * TSTEP + j * 1024);
  SCHED();
  if (w == 0) {
    const int h_mat = lane / 28, h_kh = (lane % 28) / 14, h_pc = lane % 14;
    const int ho = lane < 56 ? WS_GU + ((h_mat * 8 + h_kh * 4) * FF + rg * 56 + h_pc * 4) * 4 : OOB;
    const unsigned hmask = lane < 56 ? 1023u : 0u;
    v4u hv[4];
#pragma unroll 1
    for (unsigned spins = 0; spins < SPIN_CAP; ++spins) {
      MEMFENCE();
#pragma unroll
      for (int i = 0; i < 4; ++i) hv[i] = ld16<AUX_SC1>(rW, ho, i * FF * 4);
      if (wave_ok(bad4(bad4(bad4(bad4(0u, hv[0], tg), hv[1], tg), hv[2], tg), hv[3], tg) & hmask)) break;
      __builtin_amdgcn_s_sleep(8);
    }
    const f32x4 s = (dec4(hv[0]) + dec4(hv[1])) + (dec4(hv[2]) + dec4(hv[3]));
    if (lane < 56) LDS4(L_GS + lane * 16) = s;
    MEMFENCE();
    float hval = 0.f;
    if (lane < 56) {
      const float gsum = rbf(LDS1(L_GS + lane * 4) + LDS1(L_GS + 14 * 16 + lane * 4));
      const float usum = rbf(LDS1(L_GS + 28 * 16 + lane * 4) + LDS1(L_GS + 42 * 16 + lane * 4));
      hval = rbf((gsum > 0.f ? gsum : 0.f) * usum) * rbf(1.f - gmix);
    }
    LDS1(L_HS + lane * 4) = hval;
  }
  SCHED();
  ctr_add(L, lane, 1u << 8);
  SCHED();
#pragma unroll 1
  for (unsigned spins = 0; spins < SPIN_CAP; ++spins) {
    if ((ctr_load(L) & 255u) >= 8u) break;
    __builtin_amdgcn_s_sleep(4);
  }
  MEMFENCE();
  {
    const float mcu = max_hi(LDS1(L_MS + (((lane >> 3) & 7) * 8 + (lane & 7)) * 4));
    const f32x4 mo = LDS4(L_MS + (w * 8 + (g4 & 1) * 4) * 4);
    f32x4 mn = LDS4(L_MS + ((g4 & 1) * 4) * 4);
#pragma unroll
    for (int ww = 1; ww < 8; ++ww) { const f32x4 t = LDS4(L_MS + (ww * 8 + (g4 & 1) * 4) * 4); mn = f32x4{fmaxf(mn.x, t.x), fmaxf(mn.y, t.y), fmaxf(mn.z, t.z), fmaxf(mn.w, t.w)}; }
    lsum *= __builtin_amdgcn_exp2f(mw - mcu);
    soft_tile(2, mcu); soft_tile(3, mcu);
    const f32x4 fo = {__builtin_amdgcn_exp2f(mo.x - mn.x), __builtin_amdgcn_exp2f(mo.y - mn.y), __builtin_amdgcn_exp2f(mo.z - mn.z), __builtin_amdgcn_exp2f(mo.w - mn.w)};
#pragma unroll
    for (int i = 0; i < 8; ++i) { O[i] *= fo; PIN(O[i]); }
    lsum = xrow_sum(lsum);
    if (lane < 8) { LDS1(L_LS + (w * 8 + lane) * 4) = lsum; LDS1(L_MC + lane * 4) = mcu; }
    PIN(pf[2]); PIN(pf[3]);
  }
  const int j4 = w & 3, hb = lane >> 5;
  const int hq = rg >> 1, half = rg & 1, s_ = (lane >> 3) * 8 + 2 * j4;
  int a_ow = LA(kb + ((g4 & 1) * 4 * 128 + n16 * 8 + (g4 >> 1) * 4) * 4);
  int a_or = LA(L_KB + ((2 * j4 + hb) * 128 + (lane & 31) * 4) * 4);
  int a_lr = LA(L_LS + ((lane & 7) * 8 + 2 * j4 + hb) * 4);
  int a_mc = LA(L_MC + (2 * j4 + hb) * 4);
  int a_op = WS_OP + ((((hd * 8 + 2 * j4 + hb) * 2 + ((lane & 31) >> 4)) * 64 + sp) * 64 + (lane & 15) * 4) * 4;
  int a_ml = (lane & 31) == 0 ? WS_ML + ((hd * 8 + 2 * j4 + hb) * 64 + sp) * 16 : OOB;
  int a_ma = WS_ML + (hq * 64 + lane) * 16;
  int a_mo = WS_ML + (hq * 64 + s_) * 16;
  int a_oo = WS_OP + (((hq * 2 + half) * 64 + s_) * 64 + (lane & 7) * 4) * 4;
  int a_rcw = LA(L_RC + (j4 * 64 + 4 * ((lane & 7) + 8 * (lane >> 5)) + ((lane >> 3) & 3)) * 4);
  int a_rcr = LA(L_RC + ((lane & 3) * 64 + j4 * 16 + hb * 8) * 4);
  int a_rfw = LA(L_RF + (j4 * 256 + (lane & 31) * 8 + hb * 4) * 4);
  int a_rfr = LA(L_RF + (g4 * 256 + j4 * 64 + n16 * 4) * 4);
  int a_fp = WS_FP + ((cg4 * 64 + rg) * 256 + j4 * 64 + n16 * 4 + g4) * 4;
  int a_hs = LA(L_HS + (j4 * 14 + hb * 7) * 4);
  unsigned tl_t = g4 == 0 ? tg.t0 : g4 == 1 ? tg.t1 : g4 == 2 ? tg.t2 : tg.t3;
  PIN(tl_t);
  PIN(a_ow); PIN(a_or); PIN(a_lr); PIN(a_mc); PIN(a_op); PIN(a_ml); PIN(a_ma); PIN(a_mo); PIN(a_oo); PIN(a_rcw); PIN(a_rcr); PIN(a_rfw); PIN(a_rfr); PIN(a_fp); PIN(a_hs);
  SCHED();
  if (w >= 4) __builtin_amdgcn_s_setprio(3);
  pv_tile(vt2, 2);
  SCHED();
  pv_tile(vt3, 3);
#pragma unroll
  for (int r = 0; r < 4; ++r) {
    const f32x4 v = g4 < 2 ? f32x4{O[0][r], O[1][r], O[2][r], O[3][r]} : f32x4{O[4][r], O[5][r], O[6][r], O[7][r]};
    AT4(a_ow + r * 512) = v;
  }
  ctr_add(L, lane, 1u << 16);
  if (w >= 4) {
#pragma unroll 1
    for (unsigned spins = 0; spins < SPIN_CAP; ++spins) { if (ctr_load(L) >= (8u << 16)) break; }
    MEMFENCE();
    {
      f32x4 part[8];
#pragma unroll
      for (int ww = 0; ww < 8; ++ww) part[ww] = AT4(a_or + ww * 8192);
      const float lw = AT1(a_lr), mh = AT1(a_mc);
      SCHED();
      const float Ls = sum8(lw);
      const f32x4 acc = ((part[0] + part[1]) + (part[2] + part[3])) + ((part[4] + part[5]) + (part[6] + part[7]));
      st16(rW, a_op, enc4(acc, tg));
      st16(rW, a_ml, v4u{__float_as_uint(mh), T, __float_as_uint(Ls), T});
    }
    SCHED();
    asm volatile("s_waitcnt vmcnt(0)" ::: "memory");
    v4u ma = ld16<AUX_SC1>(rW, a_ma, 0), m0 = ld16<AUX_SC1>(rW, a_mo, 0), m1 = ld16<AUX_SC1>(rW, a_mo, 16);
    v4u o0 = ld16<AUX_SC1>(rW, a_oo, 0), o1 = ld16<AUX_SC1>(rW, a_oo, 128), o2 = ld16<AUX_SC1>(rW, a_oo, 256), o3 = ld16<AUX_SC1>(rW, a_oo, 384);
    SCHED();
    ctr_add(L, lane, 1u << 24);
    SCHED();
    unsigned bad = ((((ma.y ^ T) | (ma.w ^ T)) | ((m0.y ^ T) | (m0.w ^ T))) | ((m1.y ^ T) | (m1.w ^ T))) | (bad4(bad4(bad4(bad4(0u, o0, tg), o1, tg), o2, tg), o3, tg) & 1023u);
    if (!wave_ok(bad)) {
#pragma unroll 1
      for (unsigned spins = 0; spins < SPIN_CAP; ++spins) {
        MEMFENCE();
        const bool again = bad != 0u;
        const int xa = again ? a_ma : OOB, xm2 = again ? a_mo : OOB, xo = again ? a_oo : OOB;
        const v4u na = ld16<AUX_SC1>(rW, xa, 0), n0 = ld16<AUX_SC1>(rW, xm2, 0), n1 = ld16<AUX_SC1>(rW, xm2, 16);
        const v4u p0 = ld16<AUX_SC1>(rW, xo, 0), p1 = ld16<AUX_SC1>(rW, xo, 128), p2 = ld16<AUX_SC1>(rW, xo, 256), p3 = ld16<AUX_SC1>(rW, xo, 384);
        ma = again ? na : ma; m0 = again ? n0 : m0; m1 = again ? n1 : m1; o0 = again ? p0 : o0; o1 = again ? p1 : o1; o2 = again ? p2 : o2; o3 = again ? p3 : o3;
        bad = ((((ma.y ^ T) | (ma.w ^ T)) | ((m0.y ^ T) | (m0.w ^ T))) | ((m1.y ^ T) | (m1.w ^ T))) | (bad4(bad4(bad4(bad4(0u, o0, tg), o1, tg), o2, tg), o3, tg) & 1023u);
        if (wave_ok(bad)) break;
      }
    }
    const float M = wave_max(__uint_as_float(ma.x));
    const float inv = __builtin_amdgcn_rcpf(wave_sum(__builtin_amdgcn_exp2f(__uint_as_float(ma.x) - M) * __uint_as_float(ma.z)));
    const float w0 = __builtin_amdgcn_exp2f(__uint_as_float(m0.x) - M) * inv, w1 = __builtin_amdgcn_exp2f(__uint_as_float(m1.x) - M) * inv;
    const f32x4 a0 = dec4(o0) * w0 + dec4(o2) * w1, a1 = dec4(o1) * w0 + dec4(o3) * w1;
    const float vals[8] = {a0.x, a0.y, a0.z, a0.w, a1.x, a1.y, a1.z, a1.w};
    AT1(a_rcw) = reduce8(vals, lane);
    BARRIER();
  } else {
    const int ov = ((rg * 64 + j4 * 16 + hb * 8) * 1024 + cg4 * 256 + (lane & 31) * 8) * 2;
    const int dv = ((rg * 56 + j4 * 14 + hb * 7) * 1024 + cg4 * 256 + (lane & 31) * 8) * 2;
#pragma unroll 1
    for (unsigned spins = 0; spins < SPIN_CAP; ++spins) {
      if (((ctr_load(L) >> 8) & 255u) >= 8u) break;
      __builtin_amdgcn_s_sleep(4);
    }
    MEMFENCE();
    v4u wdv[7], wov[8];
    float hr[7];
#pragma unroll
    for (int n = 0; n < 7; ++n) hr[n] = AT1(a_hs + n * 4);
#pragma unroll
    for (int n = 0; n < 7; ++n) wdv[n] = ld16<AUX_NT>(rD, hr[n] != 0.f ? dv : OOB, n * 2048);
#pragma unroll
    for (int n = 0; n < 4; ++n) wov[n] = ld16<AUX_NT>(rO, ov, n * 2048);
    SCHED();
    float accd[4];
    {
      float acc[8] = {0.f, 0.f, 0.f, 0.f, 0.f, 0.f, 0.f, 0.f};
#pragma unroll
      for (int n = 0; n < 7; ++n) fma8(acc, hr[n], wdv[n]);
#pragma unroll
      for (int j = 0; j < 4; ++j) { accd[j] = swap_add32(acc[j], acc[j + 4]); PIN(accd[j]); }
    }
    SCHED();
#pragma unroll 1
    for (unsigned spins = 0; spins < SPIN_CAP; ++spins) { if ((ctr_load(L) >> 24) >= 4u) break; __builtin_amdgcn_s_sleep(1); }
    MEMFENCE();
#pragma unroll
    for (int n = 4; n < 8; ++n) wov[n] = ld16<AUX_NT>(rO, ov, n * 2048);
    SCHED();
    unsigned wp[4][8];
#pragma unroll
    for (int pr = 0; pr < 2; ++pr)
#pragma unroll
      for (int j = 0; j < 4; ++j) { wp[pr][2 * j] = __builtin_amdgcn_perm(wov[2 * pr + 1][j], wov[2 * pr][j], 0x05040100u); wp[pr][2 * j + 1] = __builtin_amdgcn_perm(wov[2 * pr + 1][j], wov[2 * pr][j], 0x07060302u); PIN(wp[pr][2 * j]); PIN(wp[pr][2 * j + 1]); }
    float ao[8] = {0.f, 0.f, 0.f, 0.f, 0.f, 0.f, 0.f, 0.f};
#pragma unroll
    for (int j = 0; j < 8; ++j) PIN(ao[j]);
    BARRIER();
    {
      const f32x4 pa = AT4(a_rcr), pb = AT4(a_rcr + 16);
      float o[8] = {pa.x, pa.y, pa.z, pa.w, pb.x, pb.y, pb.z, pb.w};
      sum4x8(o);
#pragma unroll
      for (int pr = 0; pr < 4; ++pr) {
        const unsigned op = pkbf(o[2 * pr], o[2 * pr + 1]);
        if (pr >= 2) {
#pragma unroll
          for (int j = 0; j < 4; ++j) { wp[pr][2 * j] = __builtin_amdgcn_perm(wov[2 * pr + 1][j], wov[2 * pr][j], 0x05040100u); wp[pr][2 * j + 1] = __builtin_amdgcn_perm(wov[2 * pr + 1][j], wov[2 * pr][j], 0x07060302u); }
        }
#pragma unroll
        for (int j = 0; j < 8; ++j) ao[j] = dot2(wp[pr][j], op, ao[j]);
      }
      f32x4 v;
      v.x = fmaf(gmix, swap_add32(ao[0], ao[4]), accd[0]); v.y = fmaf(gmix, swap_add32(ao[1], ao[5]), accd[1]);
      v.z = fmaf(gmix, swap_add32(ao[2], ao[6]), accd[2]); v.w = fmaf(gmix, swap_add32(ao[3], ao[7]), accd[3]);
      AT4(a_rfw) = v;
    }
  }
  if (w >= 4) return;
  BARRIER();
  {
    const f32x4 pc = AT4(a_rfr);
    const float s = swap_add16(swap_add32(pc.x, pc.z), swap_add32(pc.y, pc.w));
    __builtin_amdgcn_raw_buffer_store_b32(enc1(s, tl_t), rW, a_fp, 0, AUX_SC1);
  }
  if (b < 32 && w <= 1) {
    if (w == 1) __builtin_amdgcn_s_sleep(5);
    const int c = b >> 3, seg = b & 7;
    const int fo = WS_FP + ((c * 64 + (lane >> 3)) * 256 + seg * 32 + (lane & 7) * 4) * 4;
    const int oo = (lane & 8) ? OOB : (c * 256 + seg * 32 + (lane & 7) * 4 + (lane >> 4)) * 2;
    v4u f[8];
    asm volatile("s_waitcnt vmcnt(0)" ::: "memory");
#pragma unroll 1
    for (unsigned spins = 0; spins < SPIN_CAP; ++spins) {
      MEMFENCE();
#pragma unroll
      for (int n = 0; n < 8; ++n) f[n] = ld16<AUX_SC1>(rW, fo, n * 8192);
      unsigned bad = 0;
#pragma unroll
      for (int n = 0; n < 8; ++n) bad = bad4(bad, f[n], tg);
      if (wave_ok(bad & 1023u)) break;
    }
    if (b == 0 && w == 0) __builtin_amdgcn_raw_buffer_store_b32(T, rW, lane == 0 ? WS_EPOCH : OOB, 0, AUX_SC1);
    const f32x4 d = ((dec4(f[0]) + dec4(f[1])) + (dec4(f[2]) + dec4(f[3]))) + ((dec4(f[4]) + dec4(f[5])) + (dec4(f[6]) + dec4(f[7])));
    const float r = reduce4(d);
    __builtin_amdgcn_raw_buffer_store_b16((unsigned short)pkbf(r, r), rOut, oo, 0, 0);
  }
}

}

extern "C" void kernel_launch(void* const* d_in, const int* in_sizes, int n_in, void* d_out, int out_size, void* d_ws, size_t ws_size, hipStream_t stream) {
  fk::Params p;
  p.x = d_in[0]; p.K = d_in[1]; p.V = d_in[2]; p.wq = d_in[3]; p.wo = d_in[4];
  p.wg = d_in[6]; p.wu = d_in[7]; p.wd = d_in[8]; p.gw = d_in[9];
  p.out = d_out; p.ws = (unsigned char*)d_ws;
  (void)hipFuncSetAttribute((const void*)fk::fused_kernel, hipFuncAttributeMaxDynamicSharedMemorySize, fk::LDS_BYTES);
  hipLaunchKernelGGL(fk::fused_kernel, dim3(fk::NWG), dim3(fk::NTHR), fk::LDS_BYTES, stream, p);
}
```

```cpp
#include <hip/hip_runtime.h>
#include <stdint.h>

namespace fk {

typedef unsigned v4u __attribute__((ext_vector_type(4)));
typedef unsigned v2u __attribute__((ext_vector_type(2)));
typedef float f32x4 __attribute__((ext_vector_type(4)));
typedef float f32x2 __attribute__((ext_vector_type(2)));
typedef __bf16 bf16x8 __attribute__((ext_vector_type(8)));
typedef __bf16 bf16x2 __attribute__((ext_vector_type(2)));
#define LAS __attribute__((address_space(3)))
#define GAS __attribute__((address_space(1)))

constexpr int NWG = 256, NTHR = 512;
constexpr int FF = 3584;
constexpr int WS_EPOCH = 0;
constexpr int WS_QP = 4096;
constexpr int WS_GU = WS_QP + 4 * 4096 * 4;
constexpr int WS_ML = WS_GU + 2 * 8 * FF * 4;
constexpr int WS_OP = WS_ML + 32 * 64 * 16;
constexpr int WS_FP = WS_OP + 32 * 2 * 64 * 64 * 4;
constexpr int WS_END = WS_FP + 4 * 64 * 256 * 4;
constexpr int L_KB = 0;
constexpr int L_RQ = 65536;
constexpr int L_RG = L_RQ + 2048;
constexpr int L_RC = L_RG + 8192;
constexpr int L_RF = L_RC + 2048;
constexpr int L_QS = L_RF + 8192;
constexpr int L_MS = L_QS + 2048;
constexpr int L_LS = L_MS + 256;
constexpr int L_MC = L_LS + 256;
constexpr int L_CT = L_MC + 32;
constexpr int L_GS = L_CT + 32;
constexpr int L_HS = L_GS + 1024;
constexpr int L_GM = L_HS + 256;
constexpr int L_END = L_GM + 32;
constexpr int LDS_BYTES = 90112;
static_assert(L_END <= LDS_BYTES, "lds");

constexpr int AUX_NT = 2, AUX_SC1 = 16;
constexpr int OOB = 0x7ffffff0;
constexpr unsigned SPIN_CAP = 1u << 18;
constexpr unsigned LDS_CAP = 1u << 21;
constexpr float SCL = 0.08838834764831845f * 1.4426950408889634f;
constexpr int PF_LINES = 125;

struct Params {
  const void *x, *K, *V, *wq, *wo, *wg, *wu, *wd, *gw;
  void* out;
  unsigned char* ws;
};

__device__ __forceinline__ float bflo(unsigned u) { return __uint_as_float(u << 16); }
__device__ __forceinline__ float bfhi(unsigned u) { return __uint_as_float(u & 0xffff0000u); }
__device__ __forceinline__ unsigned pkbf(float a, float b) { f32x2 v = {a, b}; return __builtin_bit_cast(unsigned, __builtin_convertvector(v, bf16x2)); }
__device__ __forceinline__ float rbf(float a) { return bflo(pkbf(a, 0.f)); }
template <int AUX> __device__ __forceinline__ v4u ld16(__amdgpu_buffer_rsrc_t rs, int voff, int soff) { return __builtin_bit_cast(v4u, __builtin_amdgcn_raw_buffer_load_b128(rs, voff, soff, AUX)); }
template <int AUX> __device__ __forceinline__ v2u ld8(__amdgpu_buffer_rsrc_t rs, int voff, int soff) { return __builtin_bit_cast(v2u, __builtin_amdgcn_raw_buffer_load_b64(rs, voff, soff, AUX)); }
template <int AUX> __device__ __forceinline__ unsigned ld4(__amdgpu_buffer_rsrc_t rs, int voff, int soff) { return __builtin_amdgcn_raw_buffer_load_b32(rs, voff, soff, AUX); }
__device__ __forceinline__ void st16(__amdgpu_buffer_rsrc_t rs, int voff, v4u v) { __builtin_amdgcn_raw_buffer_store_b128(v, rs, voff, 0, AUX_SC1); }
#define RSRC(p, n) __builtin_amdgcn_make_buffer_rsrc((void*)(p), 0, (n), 0x00020000)
#define SCHED() __builtin_amdgcn_sched_barrier(0)
#define BARRIER() asm volatile("s_waitcnt lgkmcnt(0)\n\ts_barrier" ::: "memory")
#define MEMFENCE() asm volatile("" ::: "memory")
#define PIN(x) asm volatile("" : "+v"(x))

__device__ __forceinline__ float swap_add32(float a, float b) {
  auto s = __builtin_amdgcn_permlane32_swap(__float_as_uint(a), __float_as_uint(b), false, false);
  return __uint_as_float(s[0]) + __uint_as_float(s[1]);
}
__device__ __forceinline__ float swap_add16(float a, float b) {
  auto s = __builtin_amdgcn_permlane16_swap(__float_as_uint(a), __float_as_uint(b), false, false);
  return __uint_as_float(s[0]) + __uint_as_float(s[1]);
}
template <int CTRL> __device__ __forceinline__ float dpp(float x) { return __builtin_bit_cast(float, __builtin_amdgcn_mov_dpp(__builtin_bit_cast(int, x), CTRL, 0xf, 0xf, true)); }
constexpr int ROR8 = 0x128, ROR4 = 0x124, ROR2 = 0x122, ROR1 = 0x121;
constexpr int XOR1 = 0xB1, XOR2 = 0x4E, HMIR = 0x141;
__device__ __forceinline__ float xrow_max(float x) {
  auto s = __builtin_amdgcn_permlane16_swap(__float_as_uint(x), __float_as_uint(x), false, false);
  x = fmaxf(__uint_as_float(s[0]), __uint_as_float(s[1]));
  auto t = __builtin_amdgcn_permlane32_swap(__float_as_uint(x), __float_as_uint(x), false, false);
  return fmaxf(__uint_as_float(t[0]), __uint_as_float(t[1]));
}
__device__ __forceinline__ float xrow_sum(float x) {
  auto s = __builtin_amdgcn_permlane16_swap(__float_as_uint(x), __float_as_uint(x), false, false);
  x = __uint_as_float(s[0]) + __uint_as_float(s[1]);
  auto t = __builtin_amdgcn_permlane32_swap(__float_as_uint(x), __float_as_uint(x), false, false);
  return __uint_as_float(t[0]) + __uint_as_float(t[1]);
}
__device__ __forceinline__ float sum8(float x) { x += dpp<XOR1>(x); x += dpp<XOR2>(x); return x + dpp<HMIR>(x); }
__device__ __forceinline__ float sum_hi(float x) { x += dpp<ROR8>(x); return xrow_sum(x); }
__device__ __forceinline__ float max_hi(float x) { x = fmaxf(x, dpp<ROR8>(x)); return xrow_max(x); }
__device__ __forceinline__ float wave_sum(float x) { x += dpp<ROR4>(x); x += dpp<ROR2>(x); x += dpp<ROR1>(x); return sum_hi(x); }
__device__ __forceinline__ float wave_max(float x) { x = fmaxf(x, dpp<ROR4>(x)); x = fmaxf(x, dpp<ROR2>(x)); x = fmaxf(x, dpp<ROR1>(x)); return max_hi(x); }
__device__ __forceinline__ void sum4x8(float (&o)[8]) {
#define DPP8(M) "v_add_f32_dpp %0, %0, %0 " M " row_mask:0xf bank_mask:0xf\n\tv_add_f32_dpp %1, %1, %1 " M " row_mask:0xf bank_mask:0xf\n\tv_add_f32_dpp %2, %2, %2 " M " row_mask:0xf bank_mask:0xf\n\t" \
                "v_add_f32_dpp %3, %3, %3 " M " row_mask:0xf bank_mask:0xf\n\tv_add_f32_dpp %4, %4, %4 " M " row_mask:0xf bank_mask:0xf\n\tv_add_f32_dpp %5, %5, %5 " M " row_mask:0xf bank_mask:0xf\n\t" \
                "v_add_f32_dpp %6, %6, %6 " M " row_mask:0xf bank_mask:0xf\n\tv_add_f32_dpp %7, %7, %7 " M " row_mask:0xf bank_mask:0xf\n\t"
  asm volatile("s_nop 1\n\t" DPP8("quad_perm:[1,0,3,2]") DPP8("quad_perm:[2,3,0,1]") "s_nop 1" : "+v"(o[0]), "+v"(o[1]), "+v"(o[2]), "+v"(o[3]), "+v"(o[4]), "+v"(o[5]), "+v"(o[6]), "+v"(o[7]));
#undef DPP8
}
__device__ __forceinline__ float reduce4(f32x4 v) {
  const float x = swap_add16(swap_add32(v.x, v.z), swap_add32(v.y, v.w));
  return x + dpp<ROR8>(x);
}
__device__ __forceinline__ float reduce8(const float (&a)[8], int lane) {
  float t[4], u[2];
#pragma unroll
  for (int j = 0; j < 4; ++j) t[j] = swap_add32(a[j], a[j + 4]);
#pragma unroll
  for (int j = 0; j < 2; ++j) u[j] = swap_add16(t[j], t[j + 2]);
  const float v0 = u[0] + dpp<ROR8>(u[0]), v1 = u[1] + dpp<ROR8>(u[1]);
  return (lane & 8) ? v1 : v0;
}
__device__ __forceinline__ void fma8(float (&acc)[8], float xv, v4u w) {
#pragma unroll
  for (int j = 0; j < 4; ++j) { acc[2 * j] = fmaf(xv, bflo(w[j]), acc[2 * j]); acc[2 * j + 1] = fmaf(xv, bfhi(w[j]), acc[2 * j + 1]); }
}
__device__ __forceinline__ float dot2(unsigned a, unsigned b, float c) { return __builtin_amdgcn_fdot2_f32_bf16(__builtin_bit_cast(bf16x2, a), __builtin_bit_cast(bf16x2, b), c, false); }
__device__ __forceinline__ f32x4 mfma(v4u a, v4u b, f32x4 c) { return __builtin_amdgcn_mfma_f32_16x16x32_bf16(__builtin_bit_cast(bf16x8, a), __builtin_bit_cast(bf16x8, b), c, 0, 0, 0); }

struct Tags { unsigned t0, t1, t2, t3; };
__device__ __forceinline__ unsigned enc1(float v, unsigned t) { return ((__float_as_uint(v) + 512u) & ~1023u) | t; }
__device__ __forceinline__ v4u enc4(f32x4 v, const Tags& t) { return v4u{enc1(v.x, t.t0), enc1(v.y, t.t1), enc1(v.z, t.t2), enc1(v.w, t.t3)}; }
__device__ __forceinline__ unsigned orx(unsigned acc, unsigned x, unsigned t) { return __builtin_amdgcn_bitop3_b32(acc, x, t, 0xF6); }
__device__ __forceinline__ unsigned bad4(unsigned acc, v4u v, const Tags& t) { return orx(orx(orx(orx(acc, v.x, t.t0), v.y, t.t1), v.z, t.t2), v.w, t.t3); }
__device__ __forceinline__ f32x4 dec4(v4u v) { return __builtin_bit_cast(f32x4, v & ~1023u); }
__device__ __forceinline__ bool wave_ok(unsigned bad) { return __builtin_amdgcn_ballot_w64(bad != 0u) == 0ull; }

__device__ __forceinline__ void ctr_add(LAS unsigned char* L, int lane, unsigned v) {
  if (lane == 0) asm volatile("ds_add_u32 %0, %1" :: "v"((unsigned)(size_t)(L + L_CT)), "v"(v) : "memory");
}
__device__ __forceinline__ unsigned ctr_load(LAS unsigned char* L) { return __builtin_amdgcn_readfirstlane(__hip_atomic_load((LAS unsigned*)(L + L_CT), __ATOMIC_RELAXED, __HIP_MEMORY_SCOPE_WORKGROUP)); }
#define LDS4(off) (*(LAS f32x4*)(L + (off)))
#define LDSU4(off) (*(LAS v4u*)(L + (off)))
#define LDS1(off) (*(LAS float*)(L + (off)))
#define LA(off) ((int)(unsigned)(size_t)(L + (off)))
#define AT4(a) (*(LAS f32x4*)(size_t)(unsigned)(a))
#define AT1(a) (*(LAS float*)(size_t)(unsigned)(a))

__global__ void __launch_bounds__(NTHR, 2) fused_kernel(Params p) {
  extern __shared__ __attribute__((aligned(16))) unsigned char smem[];
  LAS unsigned char* L = (LAS unsigned char*)smem;
  const int tid = threadIdx.x, lane = tid & 63, w = __builtin_amdgcn_readfirstlane(tid >> 6), b = blockIdx.x;
  unsigned pf0 = 0;
  if (tid < PF_LINES) pf0 = ((const GAS unsigned*)(__builtin_amdgcn_s_getpc() & ~255ull))[tid * 32];
  SCHED();
  const __amdgpu_buffer_rsrc_t rX = RSRC(p.x, 2048), rGW = RSRC(p.gw, 2048), rK = RSRC(p.K, 64 << 20), rV = RSRC(p.V, 64 << 20), rQ = RSRC(p.wq, 8 << 20), rO = RSRC(p.wo, 8 << 20),
                                rG = RSRC(p.wg, 7 << 20), rU = RSRC(p.wu, 7 << 20), rD = RSRC(p.wd, 7 << 20), rW = RSRC(p.ws, WS_END), rOut = RSRC(p.out, 2048);
  const int q_cg = b & 63, q_ks = b >> 6;
  const bool mlp = b < 224;
  const int g_cg = b % 28, g_ks = b / 28;
  const int hd = (b >> 3) & 3, sp = (b >> 5) * 8 + (b & 7);
  const int rg = (b & 7) + 8 * (b >> 5), cg4 = (b >> 3) & 3;

  const int qv = ((q_ks * 256 + w * 32 + (lane >> 3) * 4) * 4096 + q_cg * 64 + (lane & 7) * 8) * 2;
  v4u wqv[4];
#pragma unroll
  for (int n = 0; n < 4; ++n) wqv[n] = ld16<AUX_NT>(rQ, qv, n * 8192);
  SCHED();
  const v2u xq = ld8<0>(rX, (q_ks * 256 + w * 32 + (lane >> 3) * 4) * 2, 0);
  int vz = 0;
  PIN(vz);
  const unsigned E = ld4<AUX_SC1>(rW, vz, WS_EPOCH);
  const v2u xg = ld8<0>(rX, ((g_ks & 7) * 128 + w * 16 + (lane >> 4) * 4) * 2, 0);
  const unsigned xm = ld4<0>(rX, (w * 128 + lane * 2) * 2, 0), gm = ld4<0>(rGW, (w * 128 + lane * 2) * 2, 0);
  const int gv = mlp ? ((g_ks * 128 + w * 16 + (lane >> 4) * 4) * FF + g_cg * 128 + (lane & 15) * 8) * 2 : OOB;
  v4u gtv[4], upv[4];
  SCHED();
  asm volatile("s_barrier" ::: "memory");
#pragma unroll
  for (int n = 0; n < 4; ++n) gtv[n] = ld16<AUX_NT>(rG, gv, n * FF * 2);
  SCHED();
  {
    float acc[8] = {0.f, 0.f, 0.f, 0.f, 0.f, 0.f, 0.f, 0.f};
    fma8(acc, bflo(xq.x), wqv[0]); fma8(acc, bfhi(xq.x), wqv[1]); fma8(acc, bflo(xq.y), wqv[2]); fma8(acc, bfhi(xq.y), wqv[3]);
    const float r = reduce8(acc, lane);
    LDS1(L_RQ + (w * 64 + (lane & 7) * 8 + (lane >> 3)) * 4) = r;
  }
  SCHED();
#pragma unroll
  for (int n = 0; n < 4; ++n) upv[n] = ld16<AUX_NT>(rU, gv, n * FF * 2);
  SCHED();
  const unsigned T = E + 1u;
  Tags tg;
  tg.t0 = T & 1023u; tg.t1 = (T + (T >> 10)) & 1023u; tg.t2 = (T + (T >> 20)) & 1023u; tg.t3 = (T + (T >> 30) + 341u) & 1023u;
  {
    const float s = wave_sum(rbf(bflo(xm) * bflo(gm)) + rbf(bfhi(xm) * bfhi(gm)));
    if (lane == 0) LDS1(L_GM + w * 4) = s;
    if (tid == 0) *(LAS unsigned*)(L + L_CT) = 0u;
  }
  asm volatile("" :: "v"(pf0));
  BARRIER();
  if (w == 0) {
    f32x4 s = LDS4(L_RQ + (lane & 15) * 16);
#pragma unroll
    for (int ww = 1; ww < 8; ++ww) s += LDS4(L_RQ + ww * 256 + (lane & 15) * 16);
    st16(rW, lane < 16 ? WS_QP + (q_ks * 4096 + q_cg * 64 + lane * 4) * 4 : OOB, enc4(s, tg));
  }
  float gmix;
  {
    const f32x4 a = LDS4(L_GM), c = LDS4(L_GM + 16);
    const float sg = rbf(((a.x + a.y) + (a.z + a.w)) + ((c.x + c.y) + (c.z + c.w)));
    gmix = rbf(1.f / (1.f + __expf(-sg)));
  }
  const int kvb = hd * (16 << 20) + (sp * 8 + w) * 8192;
  constexpr int TSTEP = 512 * 8192;
  const int lv = lane * 16;
  v4u kt0[8], kt1[8], kt2[8];
  {
    float ag[8] = {0.f, 0.f, 0.f, 0.f, 0.f, 0.f, 0.f, 0.f}, au[8] = {0.f, 0.f, 0.f, 0.f, 0.f, 0.f, 0.f, 0.f};
    fma8(ag, bflo(xg.x), gtv[0]); fma8(ag, bfhi(xg.x), gtv[1]); fma8(ag, bflo(xg.y), gtv[2]); fma8(ag, bfhi(xg.y), gtv[3]);
    SCHED();
#pragma unroll
    for (int j = 0; j < 4; ++j) kt0[j] = ld16<AUX_NT>(rK, lv, kvb + j * 1024);
    SCHED();
    fma8(au, bflo(xg.x), upv[0]); fma8(au, bfhi(xg.x), upv[1]); fma8(au, bflo(xg.y), upv[2]); fma8(au, bfhi(xg.y), upv[3]);
    SCHED();
#pragma unroll
    for (int j = 4; j < 8; ++j) kt0[j] = ld16<AUX_NT>(rK, lv, kvb + j * 1024);
    SCHED();
    float t[4], u[2];
#pragma unroll
    for (int j = 0; j < 4; ++j) t[j] = swap_add32(ag[j], ag[j + 4]);
#pragma unroll
    for (int j = 0; j < 2; ++j) u[j] = swap_add16(t[j], t[j + 2]);
    *(LAS f32x2*)(L + L_RG + ((w * 2 + 0) * 128 + (lane & 15) * 8 + (lane >> 4) * 2) * 4) = f32x2{u[0], u[1]};
#pragma unroll
    for (int j = 0; j < 4; ++j) t[j] = swap_add32(au[j], au[j + 4]);
#pragma unroll
    for (int j = 0; j < 2; ++j) u[j] = swap_add16(t[j], t[j + 2]);
    *(LAS f32x2*)(L + L_RG + ((w * 2 + 1) * 128 + (lane & 15) * 8 + (lane >> 4) * 2) * 4) = f32x2{u[0], u[1]};
  }
  BARRIER();
  if (w == 1) {
    const int mat = lane >> 5, pc = lane & 31;
    f32x4 s = LDS4(L_RG + (mat * 128 + pc * 4) * 4);
#pragma unroll
    for (int ww = 1; ww < 8; ++ww) s += LDS4(L_RG + ((ww * 2 + mat) * 128 + pc * 4) * 4);
    st16(rW, mlp ? WS_GU + ((mat * 8 + g_ks) * FF + g_cg * 128 + pc * 4) * 4 : OOB, enc4(s, tg));
  }
  {
    const int qo = WS_QP + ((lane >> 5) * 2 * 4096 + (hd * 8 + w) * 128 + (lane & 31) * 4) * 4;
    v4u q0 = ld16<AUX_SC1>(rW, qo, 0), q1 = ld16<AUX_SC1>(rW, qo, 4096 * 4);
    SCHED();
#pragma unroll
    for (int j = 0; j < 8; ++j) kt1[j] = ld16<AUX_NT>(rK, lv, kvb + TSTEP + j * 1024);
#pragma unroll
    for (int j = 0; j < 8; ++j) kt2[j] = ld16<AUX_NT>(rK, lv, kvb + 2 * TSTEP + j * 1024);
    SCHED();
    if (!wave_ok(bad4(bad4(0u, q0, tg), q1, tg) & 1023u)) {
#pragma unroll 1
      for (unsigned spins = 0; spins < SPIN_CAP; ++spins) {
        __builtin_amdgcn_s_sleep(1);
        MEMFENCE();
        q0 = ld16<AUX_SC1>(rW, qo, 0); q1 = ld16<AUX_SC1>(rW, qo, 4096 * 4);
        if (wave_ok(bad4(bad4(0u, q0, tg), q1, tg) & 1023u)) break;
      }
    }
    const f32x4 a = dec4(q0) + dec4(q1);
    f32x4 s;
    s.x = swap_add32(a.x, a.x); s.y = swap_add32(a.y, a.y); s.z = swap_add32(a.z, a.z); s.w = swap_add32(a.w, a.w);
    if (lane < 32) *(LAS v2u*)(L + L_QS + (w * 128 + lane * 4) * 2) = v2u{pkbf(s.x, s.y), pkbf(s.z, s.w)};
  }
  BARRIER();

  const int g4 = lane >> 4, n16 = lane & 15;
  v4u qf[4];
#pragma unroll
  for (int s = 0; s < 4; ++s) qf[s] = LDSU4(L_QS + ((n16 & 7) * 128 + (4 * s + g4) * 8) * 2);
  f32x4 S[8];
  const int kb = L_KB + w * 8192;
  auto qk_tile = [&](const v4u (&kt)[8], int t) {
#pragma unroll
    for (int j = 0; j < 8; ++j) { const int r = 4 * g4 + (j & 3); LDSU4(kb + ((j >> 2) * 16 + r) * 256 + ((n16 ^ r) << 4)) = kt[j]; }
#pragma unroll
    for (int T2 = 0; T2 < 2; ++T2) {
      f32x4 c = {0.f, 0.f, 0.f, 0.f};
#pragma unroll
      for (int s = 0; s < 4; ++s) c = mfma(LDSU4(kb + (T2 * 16 + n16) * 256 + (((4 * s + g4) ^ n16) << 4)), qf[s], c);
      S[2 * t + T2] = c;
    }
  };
  v4u kt3[8], vt0[8], vt1[8], vt2[8], vt3[8];
  qk_tile(kt0, 0);
  SCHED();
#pragma unroll
  for (int j = 0; j < 8; ++j) kt3[j] = ld16<AUX_NT>(rK, lv, kvb + 3 * TSTEP + j * 1024);
  SCHED();
  qk_tile(kt1, 1);
  SCHED();
#pragma unroll
  for (int j = 0; j < 8; ++j) vt0[j] = ld16<AUX_NT>(rV, lv, kvb + j * 1024);
  SCHED();
  qk_tile(kt2, 2);
  SCHED();
#pragma unroll
  for (int j = 0; j < 8; ++j) vt1[j] = ld16<AUX_NT>(rV, lv, kvb + TSTEP + j * 1024);
  SCHED();
  qk_tile(kt3, 3);
  SCHED();
  float mw;
  {
    float mx = fmaxf(fmaxf(S[0].x, S[0].y), fmaxf(S[0].z, S[0].w));
#pragma unroll
    for (int i = 1; i < 8; ++i) mx = fmaxf(mx, fmaxf(fmaxf(S[i].x, S[i].y), fmaxf(S[i].z, S[i].w)));
    mw = xrow_max(mx) * SCL;
    if (lane < 8) LDS1(L_MS + (w * 8 + lane) * 4) = mw;
    ctr_add(L, lane, 1u);
  }
  float lsum = 0.f;
  v4u pf[4];
  auto soft_tile = [&](int t, float ref) {
    float e[8];
#pragma unroll
    for (int i = 0; i < 4; ++i) { e[i] = __builtin_amdgcn_exp2f(fmaf(S[2 * t][i], SCL, -ref)); e[4 + i] = __builtin_amdgcn_exp2f(fmaf(S[2 * t + 1][i], SCL, -ref)); }
    lsum += ((e[0] + e[1]) + (e[2] + e[3])) + ((e[4] + e[5]) + (e[6] + e[7]));
    pf[t] = v4u{pkbf(e[0], e[1]), pkbf(e[2], e[3]), pkbf(e[4], e[5]), pkbf(e[6], e[7])};
  };
  soft_tile(0, mw); soft_tile(1, mw);
  f32x4 O[8];
#pragma unroll
  for (int i = 0; i < 8; ++i) O[i] = f32x4{0.f, 0.f, 0.f, 0.f};
  auto pv_tile = [&](const v4u (&vt)[8], int t) {
#pragma unroll
    for (int i = 0; i < 8; ++i) {
      const unsigned sel = (i & 1) ? 0x07060302u : 0x05040100u;
      const v4u bf = {__builtin_amdgcn_perm(vt[1][i >> 1], vt[0][i >> 1], sel), __builtin_amdgcn_perm(vt[3][i >> 1], vt[2][i >> 1], sel),
                      __builtin_amdgcn_perm(vt[5][i >> 1], vt[4][i >> 1], sel), __builtin_amdgcn_perm(vt[7][i >> 1], vt[6][i >> 1], sel)};
      O[i] = mfma(pf[t], bf, O[i]);
    }
  };
  pv_tile(vt0, 0);
  SCHED();
#pragma unroll
  for (int j = 0; j < 8; ++j) vt2[j] = ld16<AUX_NT>(rV, lv, kvb + 2 * TSTEP + j * 1024);
  SCHED();
  pv_tile(vt1, 1);
  SCHED();
#pragma unroll
  for (int j = 0; j < 8; ++j) vt3[j] = ld16<AUX_NT>(rV, lv, kvb + 3 * TSTEP + j * 1024);
  SCHED();
  if (w == 0) {
    const int h_mat = lane / 28, h_kh = (lane % 28) / 14, h_pc = lane % 14;
    const int ho = lane < 56 ? WS_GU + ((h_mat * 8 + h_kh * 4) * FF + rg * 56 + h_pc * 4) * 4 : OOB;
    const unsigned hmask = lane < 56 ? 1023u : 0u;
    v4u hv[4];
#pragma unroll 1
    for (unsigned spins = 0; spins < SPIN_CAP; ++spins) {
      MEMFENCE();
#pragma unroll
      for (int i = 0; i < 4; ++i) hv[i] = ld16<AUX_SC1>(rW, ho, i * FF * 4);
      if (wave_ok(bad4(bad4(bad4(bad4(0u, hv[0], tg), hv[1], tg), hv[2], tg), hv[3], tg) & hmask)) break;
      __builtin_amdgcn_s_sleep(8);
    }
    const f32x4 s = (dec4(hv[0]) + dec4(hv[1])) + (dec4(hv[2]) + dec4(hv[3]));
    if (lane < 56) LDS4(L_GS + lane * 16) = s;
    MEMFENCE();
    float hval = 0.f;
    if (lane < 56) {
      const float gsum = rbf(LDS1(L_GS + lane * 4) + LDS1(L_GS + 14 * 16 + lane * 4));
      const float usum = rbf(LDS1(L_GS + 28 * 16 + lane * 4) + LDS1(L_GS + 42 * 16 + lane * 4));
      hval = rbf((gsum > 0.f ? gsum : 0.f) * usum) * rbf(1.f - gmix);
    }
    LDS1(L_HS + lane * 4) = hval;
  }
  SCHED();
  ctr_add(L, lane, 1u << 8);
  SCHED();
#pragma unroll 1
  for (unsigned spins = 0; spins < LDS_CAP; ++spins) {
    if ((ctr_load(L) & 255u) >= 8u) break;
    __builtin_amdgcn_s_sleep(4);
  }
  MEMFENCE();
  {
    const float mcu = max_hi(LDS1(L_MS + (((lane >> 3) & 7) * 8 + (lane & 7)) * 4));
    const f32x4 mo = LDS4(L_MS + (w * 8 + (g4 & 1) * 4) * 4);
    f32x4 mn = LDS4(L_MS + ((g4 & 1) * 4) * 4);
#pragma unroll
    for (int ww = 1; ww < 8; ++ww) { const f32x4 t = LDS4(L_MS + (ww * 8 + (g4 & 1) * 4) * 4); mn = f32x4{fmaxf(mn.x, t.x), fmaxf(mn.y, t.y), fmaxf(mn.z, t.z), fmaxf(mn.w, t.w)}; }
    lsum *= __builtin_amdgcn_exp2f(mw - mcu);
    soft_tile(2, mcu); soft_tile(3, mcu);
    const f32x4 fo = {__builtin_amdgcn_exp2f(mo.x - mn.x), __builtin_amdgcn_exp2f(mo.y - mn.y), __builtin_amdgcn_exp2f(mo.z - mn.z), __builtin_amdgcn_exp2f(mo.w - mn.w)};
#pragma unroll
    for (int i = 0; i < 8; ++i) { O[i] *= fo; PIN(O[i]); }
    lsum = xrow_sum(lsum);
    if (lane < 8) { LDS1(L_LS + (w * 8 + lane) * 4) = lsum; LDS1(L_MC + lane * 4) = mcu; }
    PIN(pf[2]); PIN(pf[3]);
  }
  const int j4 = w & 3, hb = lane >> 5;
  const int hq = rg >> 1, half = rg & 1, s_ = (lane >> 3) * 8 + 2 * j4;
  int a_ow = LA(kb + ((g4 & 1) * 4 * 128 + n16 * 8 + (g4 >> 1) * 4) * 4);
  int a_or = LA(L_KB + ((2 * j4 + hb) * 128 + (lane & 31) * 4) * 4);
  int a_lr = LA(L_LS + ((lane & 7) * 8 + 2 * j4 + hb) * 4);
  int a_mc = LA(L_MC + (2 * j4 + hb) * 4);
  int a_op = WS_OP + ((((hd * 8 + 2 * j4 + hb) * 2 + ((lane & 31) >> 4)) * 64 + sp) * 64 + (lane & 15) * 4) * 4;
  int a_ml = (lane & 31) == 0 ? WS_ML + ((hd * 8 + 2 * j4 + hb) * 64 + sp) * 16 : OOB;
  int a_ma = WS_ML + (hq * 64 + lane) * 16;
  int a_mo = WS_ML + (hq * 64 + s_) * 16;
  int a_oo = WS_OP + (((hq * 2 + half) * 64 + s_) * 64 + (lane & 7) * 4) * 4;
  int a_rcw = LA(L_RC + (j4 * 64 + 4 * ((lane & 7) + 8 * (lane >> 5)) + ((lane >> 3) & 3)) * 4);
  int a_rcr = LA(L_RC + ((lane & 3) * 64 + j4 * 16 + hb * 8) * 4);
  int a_rfw = LA(L_RF + (j4 * 256 + (lane & 31) * 8 + hb * 4) * 4);
  int a_rfr = LA(L_RF + (g4 * 256 + j4 * 64 + n16 * 4) * 4);
  int a_fp = WS_FP + ((cg4 * 64 + rg) * 256 + j4 * 64 + n16 * 4 + g4) * 4;
  int a_hs = LA(L_HS + (j4 * 14 + hb * 7) * 4);
  unsigned tl_t = g4 == 0 ? tg.t0 : g4 == 1 ? tg.t1 : g4 == 2 ? tg.t2 : tg.t3;
  PIN(tl_t);
  PIN(a_ow); PIN(a_or); PIN(a_lr); PIN(a_mc); PIN(a_op); PIN(a_ml); PIN(a_ma); PIN(a_mo); PIN(a_oo); PIN(a_rcw); PIN(a_rcr); PIN(a_rfw); PIN(a_rfr); PIN(a_fp); PIN(a_hs);
  SCHED();
  if (w >= 4) __builtin_amdgcn_s_setprio(3);
  pv_tile(vt2, 2);
  SCHED();
  pv_tile(vt3, 3);
#pragma unroll
  for (int r = 0; r < 4; ++r) {
    const f32x4 v = g4 < 2 ? f32x4{O[0][r], O[1][r], O[2][r], O[3][r]} : f32x4{O[4][r], O[5][r], O[6][r], O[7][r]};
    AT4(a_ow + r * 512) = v;
  }
  ctr_add(L, lane, 1u << 16);
  if (w >= 4) {
#pragma unroll 1
    for (unsigned spins = 0; spins < LDS_CAP; ++spins) { if (ctr_load(L) >= (8u << 16)) break; }
    MEMFENCE();
    {
      f32x4 part[8];
#pragma unroll
      for (int ww = 0; ww < 8; ++ww) part[ww] = AT4(a_or + ww * 8192);
      const float lw = AT1(a_lr), mh = AT1(a_mc);
      SCHED();
      const float Ls = sum8(lw);
      const f32x4 acc = ((part[0] + part[1]) + (part[2] + part[3])) + ((part[4] + part[5]) + (part[6] + part[7]));
      st16(rW, a_op, enc4(acc, tg));
      st16(rW, a_ml, v4u{__float_as_uint(mh), T, __float_as_uint(Ls), T});
    }
    SCHED();
    asm volatile("s_waitcnt vmcnt(0)" ::: "memory");
    v4u ma = ld16<AUX_SC1>(rW, a_ma, 0), m0 = ld16<AUX_SC1>(rW, a_mo, 0), m1 = ld16<AUX_SC1>(rW, a_mo, 16);
    v4u o0 = ld16<AUX_SC1>(rW, a_oo, 0), o1 = ld16<AUX_SC1>(rW, a_oo, 128), o2 = ld16<AUX_SC1>(rW, a_oo, 256), o3 = ld16<AUX_SC1>(rW, a_oo, 384);
    SCHED();
    ctr_add(L, lane, 1u << 24);
    SCHED();
    unsigned bad = ((((ma.y ^ T) | (ma.w ^ T)) | ((m0.y ^ T) | (m0.w ^ T))) | ((m1.y ^ T) | (m1.w ^ T))) | (bad4(bad4(bad4(bad4(0u, o0, tg), o1, tg), o2, tg), o3, tg) & 1023u);
    if (!wave_ok(bad)) {
#pragma unroll 1
      for (unsigned spins = 0; spins < SPIN_CAP; ++spins) {
        MEMFENCE();
        const bool again = bad != 0u;
        const int xa = again ? a_ma : OOB, xm2 = again ? a_mo : OOB, xo = again ? a_oo : OOB;
        const v4u na = ld16<AUX_SC1>(rW, xa, 0), n0 = ld16<AUX_SC1>(rW, xm2, 0), n1 = ld16<AUX_SC1>(rW, xm2, 16);
        const v4u p0 = ld16<AUX_SC1>(rW, xo, 0), p1 = ld16<AUX_SC1>(rW, xo, 128), p2 = ld16<AUX_SC1>(rW, xo, 256), p3 = ld16<AUX_SC1>(rW, xo, 384);
        ma = again ? na : ma; m0 = again ? n0 : m0; m1 = again ? n1 : m1; o0 = again ? p0 : o0; o1 = again ? p1 : o1; o2 = again ? p2 : o2; o3 = again ? p3 : o3;
        bad = ((((ma.y ^ T) | (ma.w ^ T)) | ((m0.y ^ T) | (m0.w ^ T))) | ((m1.y ^ T) | (m1.w ^ T))) | (bad4(bad4(bad4(bad4(0u, o0, tg), o1, tg), o2, tg), o3, tg) & 1023u);
        if (wave_ok(bad)) break;
      }
    }
    const float M = wave_max(__uint_as_float(ma.x));
    const float inv = __builtin_amdgcn_rcpf(wave_sum(__builtin_amdgcn_exp2f(__uint_as_float(ma.x) - M) * __uint_as_float(ma.z)));
    const float w0 = __builtin_amdgcn_exp2f(__uint_as_float(m0.x) - M) * inv, w1 = __builtin_amdgcn_exp2f(__uint_as_float(m1.x) - M) * inv;
    const f32x4 a0 = dec4(o0) * w0 + dec4(o2) * w1, a1 = dec4(o1) * w0 + dec4(o3) * w1;
    const float vals[8] = {a0.x, a0.y, a0.z, a0.w, a1.x, a1.y, a1.z, a1.w};
    AT1(a_rcw) = reduce8(vals, lane);
    BARRIER();
  } else {
    const int ov = ((rg * 64 + j4 * 16 + hb * 8) * 1024 + cg4 * 256 + (lane & 31) * 8) * 2;
    const int dv = ((rg * 56 + j4 * 14 + hb * 7) * 1024 + cg4 * 256 + (lane & 31) * 8) * 2;
#pragma unroll 1
    for (unsigned spins = 0; spins < LDS_CAP; ++spins) {
      if (((ctr_load(L) >> 8) & 255u) >= 8u) break;
      __builtin_amdgcn_s_sleep(4);
    }
    MEMFENCE();
    v4u wdv[7], wov[8];
    float hr[7];
#pragma unroll
    for (int n = 0; n < 7; ++n) hr[n] = AT1(a_hs + n * 4);
#pragma unroll
    for (int n = 0; n < 7; ++n) wdv[n] = ld16<AUX_NT>(rD, hr[n] != 0.f ? dv : OOB, n * 2048);
#pragma unroll
    for (int n = 0; n < 4; ++n) wov[n] = ld16<AUX_NT>(rO, ov, n * 2048);
    SCHED();
    float accd[4];
    {
      float acc[8] = {0.f, 0.f, 0.f, 0.f, 0.f, 0.f, 0.f, 0.f};
#pragma unroll
      for (int n = 0; n < 7; ++n) fma8(acc, hr[n], wdv[n]);
#pragma unroll
      for (int j = 0; j < 4; ++j) { accd[j] = swap_add32(acc[j], acc[j + 4]); PIN(accd[j]); }
    }
    SCHED();
#pragma unroll 1
    for (unsigned spins = 0; spins < LDS_CAP; ++spins) { if ((ctr_load(L) >> 24) >= 4u) break; __builtin_amdgcn_s_sleep(1); }
    MEMFENCE();
#pragma unroll
    for (int n = 4; n < 8; ++n) wov[n] = ld16<AUX_NT>(rO, ov, n * 2048);
    SCHED();
    unsigned wp[4][8];
#pragma unroll
    for (int pr = 0; pr < 2; ++pr)
#pragma unroll
      for (int j = 0; j < 4; ++j) { wp[pr][2 * j] = __builtin_amdgcn_perm(wov[2 * pr + 1][j], wov[2 * pr][j], 0x05040100u); wp[pr][2 * j + 1] = __builtin_amdgcn_perm(wov[2 * pr + 1][j], wov[2 * pr][j], 0x07060302u); PIN(wp[pr][2 * j]); PIN(wp[pr][2 * j + 1]); }
    float ao[8] = {0.f, 0.f, 0.f, 0.f, 0.f, 0.f, 0.f, 0.f};
#pragma unroll
    for (int j = 0; j < 8; ++j) PIN(ao[j]);
    BARRIER();
    {
      const f32x4 pa = AT4(a_rcr), pb = AT4(a_rcr + 16);
      float o[8] = {pa.x, pa.y, pa.z, pa.w, pb.x, pb.y, pb.z, pb.w};
      sum4x8(o);
#pragma unroll
      for (int pr = 0; pr < 4; ++pr) {
        const unsigned op = pkbf(o[2 * pr], o[2 * pr + 1]);
        if (pr >= 2) {
#pragma unroll
          for (int j = 0; j < 4; ++j) { wp[pr][2 * j] = __builtin_amdgcn_perm(wov[2 * pr + 1][j], wov[2 * pr][j], 0x05040100u); wp[pr][2 * j + 1] = __builtin_amdgcn_perm(wov[2 * pr + 1][j], wov[2 * pr][j], 0x07060302u); }
        }
#pragma unroll
        for (int j = 0; j < 8; ++j) ao[j] = dot2(wp[pr][j], op, ao[j]);
      }
      f32x4 v;
      v.x = fmaf(gmix, swap_add32(ao[0], ao[4]), accd[0]); v.y = fmaf(gmix, swap_add32(ao[1], ao[5]), accd[1]);
      v.z = fmaf(gmix, swap_add32(ao[2], ao[6]), accd[2]); v.w = fmaf(gmix, swap_add32(ao[3], ao[7]), accd[3]);
      AT4(a_rfw) = v;
    }
  }
  if (w >= 4) return;
  BARRIER();
  {
    const f32x4 pc = AT4(a_rfr);
    const float s = swap_add16(swap_add32(pc.x, pc.z), swap_add32(pc.y, pc.w));
    __builtin_amdgcn_raw_buffer_store_b32(enc1(s, tl_t), rW, a_fp, 0, AUX_SC1);
  }
  if (b < 32 && w <= 1) {
    if (w == 1) __builtin_amdgcn_s_sleep(5);
    const int c = b >> 3, seg = b & 7;
    const int fo = WS_FP + ((c * 64 + (lane >> 3)) * 256 + seg * 32 + (lane & 7) * 4) * 4;
    const int oo = (lane & 8) ? OOB : (c * 256 + seg * 32 + (lane & 7) * 4 + (lane >> 4)) * 2;
    v4u f[8];
    asm volatile("s_waitcnt vmcnt(0)" ::: "memory");
#pragma unroll 1
    for (unsigned spins = 0; spins < SPIN_CAP; ++spins) {
      MEMFENCE();
#pragma unroll
      for (int n = 0; n < 8; ++n) f[n] = ld16<AUX_SC1>(rW, fo, n * 8192);
      unsigned bad = 0;
#pragma unroll
      for (int n = 0; n < 8; ++n) bad = bad4(bad, f[n], tg);
      if (wave_ok(bad & 1023u)) break;
    }
    if (b == 0 && w == 0) __builtin_amdgcn_raw_buffer_store_b32(T, rW, lane == 0 ? WS_EPOCH : OOB, 0, AUX_SC1);
    const f32x4 d = ((dec4(f[0]) + dec4(f[1])) + (dec4(f[2]) + dec4(f[3]))) + ((dec4(f[4]) + dec4(f[5])) + (dec4(f[6]) + dec4(f[7])));
    const float r = reduce4(d);
    __builtin_amdgcn_raw_buffer_store_b16((unsigned short)pkbf(r, r), rOut, oo, 0, 0);
  }
}

}

extern "C" void kernel_launch(void* const* d_in, const int* in_sizes, int n_in, void* d_out, int out_size, void* d_ws, size_t ws_size, hipStream_t stream) {
  fk::Params p;
  p.x = d_in[0]; p.K = d_in[1]; p.V = d_in[2]; p.wq = d_in[3]; p.wo = d_in[4];
  p.wg = d_in[6]; p.wu = d_in[7]; p.wd = d_in[8]; p.gw = d_in[9];
  p.out = d_out; p.ws = (unsigned char*)d_ws;
  (void)hipFuncSetAttribute((const void*)fk::fused_kernel, hipFuncAttributeMaxDynamicSharedMemorySize, fk::LDS_BYTES);
  hipLaunchKernelGGL(fk::fused_kernel, dim3(fk::NWG), dim3(fk::NTHR), fk::LDS_BYTES, stream, p);
}
```
